```python
import torch

def _softmax(x, axis=-1):
    return torch.softmax(x, dim=axis)
def _gelu(x):
    return torch.nn.functional.gelu(x, approximate="tanh")
def _silu(x):
    return torch.nn.functional.silu(x)
def _sigmoid(x):
    return torch.sigmoid(x)
def _layernorm(x, axis=-1, eps=1e-5):
    m = x.mean(dim=axis, keepdim=True)
    v = x.var(dim=axis, keepdim=True, unbiased=False)
    return (x - m) / torch.sqrt(v + eps)
def _scatter_add(tgt, idx, src, axis=0):
    return tgt.clone().index_add_(axis, idx, src)
def _segment_sum(src, seg, n):
    out = torch.zeros((n,) + src.shape[1:], dtype=src.dtype)
    return out.index_add_(0, seg, src)
def _pad(x, pw):
    flat = tuple(v for pair in reversed(pw) for v in pair)
    return torch.nn.functional.pad(x, flat)

kernel_name = 'standalone__mha_softmax__near_ridge'

def setup_inputs():

    _w = (lambda a: (lambda t: t.to(torch.bfloat16) if t.is_floating_point() else t)(torch.as_tensor(a)))
    B, H, Sq, Sk, D = 8, 16, 624, 1024, 256
    return {
        "q": _w((lambda *s: torch.randn(*s, dtype=torch.bfloat16) * 0.8)(B, H, Sq, D)),
        "k": _w((lambda *s: torch.randn(*s, dtype=torch.bfloat16) * 0.8)(B, H, Sk, D)),
        "v": _w((lambda *s: torch.randn(*s, dtype=torch.bfloat16) * 0.05)(B, H, Sk, D)),
    }

def reference(q, k, v):
    scale = q.shape[-1] ** -0.5
    scores = torch.einsum("bhqd,bhkd->bhqk", q, k) * scale
    p = _softmax(scores, axis=-1)
    return torch.einsum("bhqk,bhkd->bhqd", p, v)
```

```cpp
#include <hip/hip_runtime.h>
#include <hip/hip_bf16.h>

__global__ __launch_bounds__(256, 1) void mha_fwd_d256_kernel(const void* q, const void* k, const void* v, void* o) {
  __shared__ __attribute__((aligned(1024))) char lds[163840];
  asm volatile("" :: "v"(lds));
  const unsigned long long pq = (unsigned long long)q, pk = (unsigned long long)k, pv = (unsigned long long)v, po = (unsigned long long)o;
  asm volatile(
  "s_mov_b32 s62, %9\n"
  "s_mov_b32 s64, %8\n"
  "s_and_b32 s65, s64, 7\n"
  "s_lshl_b32 s65, s65, 3\n"
  "s_lshr_b32 s66, s64, 5\n"
  "s_add_u32 s65, s65, s66\n"
  "s_lshr_b32 s63, s64, 3\n"
  "s_and_b32 s63, s63, 3\n"
  "s_lshl_b32 s66, s65, 20\n"
  "s_mul_i32 s67, s65, 638976\n"
  "s_add_u32 s40, %2, s66\n"
  "s_addc_u32 s41, %3, 0\n"
  "s_and_b32 s41, s41, 0xffff\n"
  "s_mov_b32 s42, 1048576\n"
  "s_mov_b32 s43, 0x20000\n"
  "s_add_u32 s48, %0, s67\n"
  "s_addc_u32 s49, %1, 0\n"
  "s_and_b32 s49, s49, 0xffff\n"
  "s_mov_b32 s50, 319488\n"
  "s_mov_b32 s51, 0x20000\n"
  "s_lshl_b32 s61, s62, 13\n"
  "s_mul_i32 s80, s63, 81920\n"
  "v_mbcnt_lo_u32_b32 v249, -1, 0\n"
  "v_mbcnt_hi_u32_b32 v249, -1, v249\n"
  "v_lshrrev_b32 v250, 5, v249\n"
  "s_lshl_b32 s64, s62, 1\n"
  "v_or_b32 v251, s64, v250\n"
  "v_lshlrev_b32 v251, 1, v251\n"
  "v_and_b32 v242, 31, v249\n"
  "v_xor_b32 v242, v242, v251\n"
  "v_lshlrev_b32 v242, 4, v242\n"
  "v_lshl_add_u32 v242, v250, 9, v242\n"
  "v_add_u32 v242, s61, v242\n"
  "s_lshr_b32 s77, %8, 5\n"
  "s_lshl_b32 s77, s77, 16\n"
  "s_min_u32 s77, s77, 425984\n"
  "s_cmp_eq_u32 s77, 65536\n"
  "s_cselect_b32 s77, 98304, s77\n"
  "s_mov_b32 s78, 0\n"
  "s_mov_b32 s59, s77\n"
  "s_mov_b32 s95, s80\n"
  "s_add_u32 m0, s61, 65536\n"
  "s_nop 0\n"
  "buffer_load_dwordx4 v242, s[48:51], s95 offen offset:0 nt lds\n"
  "s_add_u32 m0, s61, 0\n"
  "s_nop 0\n"
  "buffer_load_dwordx4 v242, s[40:43], s59 offen offset:0 nt lds\n"
  "s_add_u32 m0, s61, 65536\n"
  "s_nop 0\n"
  "buffer_load_dwordx4 v242, s[48:51], s95 offen offset:1024 nt lds\n"
  "s_add_u32 m0, s61, 0\n"
  "s_nop 0\n"
  "buffer_load_dwordx4 v242, s[40:43], s59 offen offset:1024 nt lds\n"
  "s_add_u32 m0, s61, 65536\n"
  "s_nop 0\n"
  "buffer_load_dwordx4 v242, s[48:51], s95 offen offset:2048 nt lds\n"
  "s_add_u32 m0, s61, 0\n"
  "s_nop 0\n"
  "buffer_load_dwordx4 v242, s[40:43], s59 offen offset:2048 nt lds\n"
  "s_add_u32 m0, s61, 65536\n"
  "s_nop 0\n"
  "buffer_load_dwordx4 v242, s[48:51], s95 offen offset:3072 nt lds\n"
  "s_add_u32 m0, s61, 0\n"
  "s_nop 0\n"
  "buffer_load_dwordx4 v242, s[40:43], s59 offen offset:3072 nt lds\n"
  "s_add_u32 s95, s95, 4096\n"
  "s_add_u32 s59, s59, 4096\n"
  "s_add_u32 m0, s61, 69632\n"
  "s_nop 0\n"
  "buffer_load_dwordx4 v242, s[48:51], s95 offen offset:0 nt lds\n"
  "s_add_u32 m0, s61, 4096\n"
  "s_nop 0\n"
  "buffer_load_dwordx4 v242, s[40:43], s59 offen offset:0 nt lds\n"
  "s_add_u32 m0, s61, 69632\n"
  "s_nop 0\n"
  "buffer_load_dwordx4 v242, s[48:51], s95 offen offset:1024 nt lds\n"
  "s_add_u32 m0, s61, 4096\n"
  "s_nop 0\n"
  "buffer_load_dwordx4 v242, s[40:43], s59 offen offset:1024 nt lds\n"
  "s_add_u32 m0, s61, 69632\n"
  "s_nop 0\n"
  "buffer_load_dwordx4 v242, s[48:51], s95 offen offset:2048 nt lds\n"
  "s_add_u32 m0, s61, 4096\n"
  "s_nop 0\n"
  "buffer_load_dwordx4 v242, s[40:43], s59 offen offset:2048 nt lds\n"
  "s_add_u32 m0, s61, 69632\n"
  "s_nop 0\n"
  "buffer_load_dwordx4 v242, s[48:51], s95 offen offset:3072 nt lds\n"
  "s_add_u32 m0, s61, 4096\n"
  "s_nop 0\n"
  "buffer_load_dwordx4 v242, s[40:43], s59 offen offset:3072 nt lds\n"
  "s_add_u32 s95, s95, 28672\n"
  "s_add_u32 s59, s59, 28672\n"
  "s_and_b32 s59, s59, 0x7ffff\n"
  "s_or_b32 s59, s59, s78\n"
  "s_getpc_b64 s[84:85]\n"
  "s_and_b32 s84, s84, 0xffffff00\n"
  "s_and_b32 s85, s85, 0xffff\n"
  "s_mov_b32 s86, .Lattn_CODE1-.Lattn_CODE0\n"
  "s_mov_b32 s87, 0x20000\n"
  "s_lshr_b32 s64, %8, 3\n"
  "s_lshl_b32 s64, s64, 2\n"
  "s_add_u32 s64, s64, s62\n"
  "s_lshl_b32 s64, s64, 9\n"
  "v_lshlrev_b32 v207, 3, v249\n"
  "v_add_u32 v207, s64, v207\n"
  ".Lattn_CODE0:\n"
  "buffer_load_dword v254, v207, s[84:87], 0 offen offset:0\n"
  "s_add_u32 m0, s61, 98304\n"
  "s_nop 0\n"
  "buffer_load_dwordx4 v242, s[48:51], s95 offen offset:0 nt lds\n"
  "s_and_b32 s70, s62, 1\n"
  "s_mov_b32 s82, 4096\n"
  "s_lshr_b32 s64, s62, 1\n"
  "s_lshl_b32 s64, s64, 13\n"
  "s_lshl_b32 s65, s70, 12\n"
  "s_add_u32 s93, s64, s65\n"
  "s_add_u32 s93, s93, 147456\n"
  "s_add_u32 s65, s64, s80\n"
  "s_add_u32 s65, s65, 65536\n"
  "s_lshl_b32 s81, s70, 8\n"
  "s_add_u32 s65, s65, s81\n"
  "v_lshrrev_b32 v244, 3, v249\n"
  "v_and_b32 v245, 7, v249\n"
  "v_bfe_u32 v246, v249, 4, 2\n"
  "v_lshlrev_b32 v246, 1, v246\n"
  "v_xor_b32 v246, v246, v245\n"
  "v_lshlrev_b32 v247, 9, v244\n"
  "v_add_u32 v247, s65, v247\n"
  "v_lshl_add_u32 v190, v246, 4, v247\n"
  "v_lshl_add_u32 v189, v245, 4, v247\n"
  "s_add_u32 s44, %4, s66\n"
  "s_addc_u32 s45, %5, 0\n"
  "s_and_b32 s45, s45, 0xffff\n"
  "s_mov_b32 s46, 1048576\n"
  "s_mov_b32 s47, 0x20000\n"
  "s_add_u32 s52, %6, s67\n"
  "s_addc_u32 s53, %7, 0\n"
  "s_and_b32 s53, s53, 0xffff\n"
  "s_mov_b32 s54, 319488\n"
  "buffer_load_dwordx4 v242, s[48:51], s95 offen offset:1024 nt lds\n"
  "s_mov_b32 s55, 0x20000\n"
  "s_mov_b32 s60, s77\n"
  "s_mov_b32 s90, s40\n"
  "s_mov_b32 s91, s41\n"
  "s_add_u32 s64, s64, 147456\n"
  "v_bfe_u32 v246, v249, 1, 2\n"
  "v_lshlrev_b32 v246, 1, v246\n"
  "v_lshrrev_b32 v247, 4, v249\n"
  "v_xor_b32 v246, v246, v247\n"
  "v_lshlrev_b32 v246, 4, v246\n"
  "v_and_b32 v247, 15, v249\n"
  "v_lshl_add_u32 v246, v247, 7, v246\n"
  "v_add_u32 v197, s64, v246\n"
  "v_xor_b32 v198, 64, v197\n"
  "s_lshl_b32 s64, s62, 10\n"
  "s_add_u32 s64, s64, s80\n"
  "v_and_b32 v246, 1, v244\n"
  "v_lshrrev_b32 v247, 1, v244\n"
  "v_lshl_add_u32 v246, v247, 4, v246\n"
  "v_lshlrev_b32 v246, 9, v246\n"
  "v_lshl_add_u32 v246, v245, 4, v246\n"
  "v_add_u32 v188, s64, v246\n"
  "s_mov_b32 s81, 32768\n"
  "s_mov_b32 s92, 36864\n"
  "s_add_u32 s64, s61, 65536\n"
  "v_lshlrev_b32 v191, 4, v249\n"
  "v_add_u32 v191, s64, v191\n"
  "s_lshl_b32 s64, s62, 12\n"
  "s_add_u32 s64, s64, 131072\n"
  "buffer_load_dwordx4 v242, s[48:51], s95 offen offset:2048 nt lds\n"
  "v_xor_b32 v246, v245, v244\n"
  "v_lshlrev_b32 v246, 4, v246\n"
  "v_lshl_add_u32 v246, v244, 7, v246\n"
  "v_add_u32 v196, s64, v246\n"
  "v_and_b32 v246, 15, v249\n"
  "v_lshlrev_b32 v246, 7, v246\n"
  "v_bfe_u32 v247, v249, 4, 1\n"
  "v_lshl_add_u32 v246, v247, 3, v246\n"
  "v_add_u32 v246, s64, v246\n"
  "v_lshrrev_b32 v247, 5, v249\n"
  "v_add_u32 v244, 0, v247\n"
  "v_xor_b32 v244, v244, v245\n"
  "v_lshl_add_u32 v192, v244, 4, v246\n"
  "v_add_u32 v244, 2, v247\n"
  "v_xor_b32 v244, v244, v245\n"
  "v_lshl_add_u32 v193, v244, 4, v246\n"
  "v_add_u32 v244, 4, v247\n"
  "v_xor_b32 v244, v244, v245\n"
  "v_lshl_add_u32 v194, v244, 4, v246\n"
  "v_add_u32 v244, 6, v247\n"
  "v_xor_b32 v244, v244, v245\n"
  "v_lshl_add_u32 v195, v244, 4, v246\n"
  "s_xor_b32 s94, s61, 8192\n"
  "s_add_u32 s94, s94, 65536\n"
  "s_sub_u32 s94, s94, s64\n"
  "s_lshr_b32 s65, s62, 1\n"
  "s_add_u32 s65, s65, s63\n"
  "v_mov_b32 v148, 0\n"
  "v_mov_b32 v149, 0\n"
  "buffer_load_dwordx4 v242, s[48:51], s95 offen offset:3072 nt lds\n"
  "v_mov_b32 v150, 0\n"
  "v_mov_b32 v151, 0\n"
  "v_mov_b32 v235, 0\n"
  "v_mov_b32 v236, 0\n"
  "v_mov_b32 v237, 0\n"
  "v_mfma_f32_32x32x16_bf16 v[0:15], v[148:151], v[148:151], 0\n"
  "s_cmp_eq_u32 s65, 4\n"
  "s_cselect_b32 s89, 0x447a0000, 0\n"
  "s_lshl_b32 s64, s70, 11\n"
  "s_xor_b32 s65, s64, 2048\n"
  "s_lshl_b32 s66, s62, 10\n"
  "s_add_u32 s67, s66, 65536\n"
  "v_and_b32 v250, 15, v249\n"
  "v_lshrrev_b32 v251, 4, v249\n"
  "v_and_b32 v252, 9, v250\n"
  "v_bfe_u32 v246, v250, 1, 2\n"
  "v_mfma_f32_32x32x16_bf16 v[16:31], v[148:151], v[148:151], 0\n"
  "v_lshl_add_u32 v252, v246, 4, v252\n"
  "v_lshlrev_b32 v252, 9, v252\n"
  "v_and_b32 v248, 7, v250\n"
  "v_lshlrev_b32 v248, 1, v248\n"
  "v_add_u32 v246, 0, v251\n"
  "v_xor_b32 v246, v246, v248\n"
  "v_lshl_add_u32 v246, v246, 4, v252\n"
  "v_add_u32 v208, s64, v246\n"
  "v_add_u32 v212, s65, v246\n"
  "v_add_u32 v199, s66, v246\n"
  "v_mfma_f32_32x32x16_bf16 v[32:47], v[148:151], v[148:151], 0\n"
  "v_add_u32 v152, s67, v246\n"
  "s_add_u32 s95, s95, 4096\n"
  "s_add_u32 m0, s61, 102400\n"
  "s_nop 0\n"
  "buffer_load_dwordx4 v242, s[48:51], s95 offen offset:0 nt lds\n"
  "v_add_u32 v246, 4, v251\n"
  "v_xor_b32 v246, v246, v248\n"
  "v_lshl_add_u32 v246, v246, 4, v252\n"
  "v_add_u32 v209, s64, v246\n"
  "v_add_u32 v213, s65, v246\n"
  "v_add_u32 v200, s66, v246\n"
  "v_add_u32 v153, s67, v246\n"
  "v_add_u32 v246, 8, v251\n"
  "v_xor_b32 v246, v246, v248\n"
  "v_mfma_f32_32x32x16_bf16 v[48:63], v[148:151], v[148:151], 0\n"
  "v_lshl_add_u32 v246, v246, 4, v252\n"
  "v_add_u32 v210, s64, v246\n"
  "v_add_u32 v214, s65, v246\n"
  "v_add_u32 v201, s66, v246\n"
  "v_add_u32 v154, s67, v246\n"
  "v_add_u32 v246, 12, v251\n"
  "v_xor_b32 v246, v246, v248\n"
  "v_lshl_add_u32 v246, v246, 4, v252\n"
  "v_add_u32 v211, s64, v246\n"
  "v_add_u32 v215, s65, v246\n"
  "v_mfma_f32_32x32x16_bf16 v[64:79], v[148:151], v[148:151], 0\n"
  "v_add_u32 v202, s66, v246\n"
  "v_add_u32 v155, s67, v246\n"
  "s_add_u32 s64, s64, 65536\n"
  "s_add_u32 s65, s65, 65536\n"
  "v_lshrrev_b32 v250, 2, v249\n"
  "v_and_b32 v252, 9, v250\n"
  "v_bfe_u32 v246, v250, 1, 2\n"
  "v_lshl_add_u32 v252, v246, 4, v252\n"
  "buffer_load_dwordx4 v242, s[48:51], s95 offen offset:1024 nt lds\n"
  "v_lshlrev_b32 v252, 9, v252\n"
  "v_and_b32 v248, 7, v250\n"
  "v_mfma_f32_32x32x16_bf16 v[80:95], v[148:151], v[148:151], 0\n"
  "v_lshlrev_b32 v248, 1, v248\n"
  "v_and_b32 v246, 1, v249\n"
  "v_lshl_add_u32 v252, v246, 3, v252\n"
  "v_bfe_u32 v251, v249, 1, 1\n"
  "v_add_u32 v246, 0, v251\n"
  "v_xor_b32 v246, v246, v248\n"
  "v_lshl_add_u32 v246, v246, 4, v252\n"
  "v_add_u32 v216, s64, v246\n"
  "v_add_u32 v224, s65, v246\n"
  "v_add_u32 v246, 2, v251\n"
  "v_mfma_f32_32x32x16_bf16 v[96:111], v[148:151], v[148:151], 0\n"
  "v_xor_b32 v246, v246, v248\n"
  "v_lshl_add_u32 v246, v246, 4, v252\n"
  "v_add_u32 v217, s64, v246\n"
  "v_add_u32 v225, s65, v246\n"
  "v_add_u32 v246, 4, v251\n"
  "v_xor_b32 v246, v246, v248\n"
  "v_lshl_add_u32 v246, v246, 4, v252\n"
  "v_add_u32 v218, s64, v246\n"
  "v_add_u32 v226, s65, v246\n"
  "v_add_u32 v246, 6, v251\n"
  "v_mfma_f32_32x32x16_bf16 v[112:127], v[148:151], v[148:151], 0\n"
  "v_xor_b32 v246, v246, v248\n"
  "v_lshl_add_u32 v246, v246, 4, v252\n"
  "v_add_u32 v219, s64, v246\n"
  "v_add_u32 v227, s65, v246\n"
  "buffer_load_dwordx4 v242, s[48:51], s95 offen offset:2048 nt lds\n"
  "v_add_u32 v246, 8, v251\n"
  "v_xor_b32 v246, v246, v248\n"
  "v_lshl_add_u32 v246, v246, 4, v252\n"
  "v_add_u32 v220, s64, v246\n"
  "v_add_u32 v228, s65, v246\n"
  "v_add_u32 v246, 10, v251\n"
  "v_mfma_f32_32x32x16_bf16 a[0:15], v[148:151], v[148:151], 0\n"
  "v_xor_b32 v246, v246, v248\n"
  "v_lshl_add_u32 v246, v246, 4, v252\n"
  "v_add_u32 v221, s64, v246\n"
  "v_add_u32 v229, s65, v246\n"
  "v_add_u32 v246, 12, v251\n"
  "v_xor_b32 v246, v246, v248\n"
  "v_lshl_add_u32 v246, v246, 4, v252\n"
  "v_add_u32 v222, s64, v246\n"
  "v_add_u32 v230, s65, v246\n"
  "v_add_u32 v246, 14, v251\n"
  "v_mfma_f32_32x32x16_bf16 a[16:31], v[148:151], v[148:151], 0\n"
  "v_xor_b32 v246, v246, v248\n"
  "v_lshl_add_u32 v246, v246, 4, v252\n"
  "v_add_u32 v223, s64, v246\n"
  "v_add_u32 v231, s65, v246\n"
  "s_mov_b32 s56, 0x3db8aa3b\n"
  "s_mov_b32 s88, 0x46800000\n"
  "s_mov_b32 s57, 0x46800000\n"
  "s_mov_b32 s83, 0x7fc00000\n"
  "s_mov_b32 s58, 0\n"
  "s_mov_b32 s69, 0x00010001\n"
  "v_mfma_f32_32x32x16_bf16 a[32:47], v[148:151], v[148:151], 0\n"
  "buffer_load_dwordx4 v242, s[48:51], s95 offen offset:3072 nt lds\n"
  "s_mov_b32 s68, 0\n"
  "s_lshl_b32 s65, s62, 11\n"
  "s_add_u32 s64, s80, s65\n"
  "v_lshlrev_b32 v253, 5, v249\n"
  "v_add_u32 v253, s64, v253\n"
  "s_mov_b32 s96, s48\n"
  "s_mov_b32 s97, s49\n"
  "s_mov_b32 s98, s50\n"
  "s_mov_b32 s99, s51\n"
  "s_mov_b32 s100, 0\n"
  "v_mfma_f32_32x32x16_bf16 a[48:63], v[148:151], v[148:151], 0\n"
  "s_mov_b32 s101, 0\n"
  "s_add_u32 s96, s96, 319488\n"
  "s_addc_u32 s97, s97, 0\n"
  "s_cmp_ge_u32 s62, 2\n"
  "s_cselect_b32 s72, s44, s40\n"
  "s_cselect_b32 s73, s45, s41\n"
  "s_cselect_b32 s65, 4096, 0\n"
  "s_lshl_b32 s64, s63, 12\n"
  "s_add_u32 s64, s64, s77\n"
  "s_add_u32 s64, s64, 450560\n"
  "s_sub_u32 s64, s64, s80\n"
  "s_sub_u32 s64, s64, s65\n"
  "s_add_u32 s72, s72, s64\n"
  "s_addc_u32 s73, s73, 0\n"
  "s_sub_u32 s74, s72, 98304\n"
  "s_subb_u32 s75, s73, 0\n"
  "s_cmp_eq_u32 s77, 0\n"
  "s_cselect_b32 s64, 524288, 0\n"
  "s_add_u32 s74, s74, s64\n"
  "s_addc_u32 s75, s75, 0\n"
  "s_waitcnt vmcnt(9) lgkmcnt(0)\n"
  "s_barrier\n"
  "ds_read_b128 a[64:67], v152 offset:0\n"
  "ds_read_b128 a[68:71], v153 offset:0\n"
  "ds_read_b128 a[72:75], v154 offset:0\n"
  "ds_read_b128 a[76:79], v155 offset:0\n"
  "ds_read_b128 a[80:83], v152 offset:256\n"
  "ds_read_b128 a[84:87], v153 offset:256\n"
  "ds_read_b128 a[88:91], v154 offset:256\n"
  "ds_read_b128 a[92:95], v155 offset:256\n"
  "ds_read_b128 a[160:163], v208 offset:0\n"
  "ds_read_b128 a[164:167], v209 offset:0\n"
  "ds_read_b128 a[168:171], v210 offset:0\n"
  "ds_read_b128 a[172:175], v211 offset:0\n"
  "ds_read_b128 a[176:179], v208 offset:256\n"
  "ds_read_b128 a[180:183], v209 offset:256\n"
  "ds_read_b128 a[184:187], v210 offset:256\n"
  "ds_read_b128 a[188:191], v211 offset:256\n"
  "ds_read_b128 a[192:195], v208 offset:1024\n"
  "ds_read_b128 a[196:199], v209 offset:1024\n"
  "ds_read_b128 a[200:203], v210 offset:1024\n"
  "ds_read_b128 a[204:207], v211 offset:1024\n"
  "ds_read_b128 a[208:211], v208 offset:1280\n"
  "ds_read_b128 a[212:215], v209 offset:1280\n"
  "ds_read_b128 a[216:219], v210 offset:1280\n"
  "ds_read_b128 a[220:223], v211 offset:1280\n"
  "s_waitcnt lgkmcnt(15)\n"
  "v_mfma_f32_16x16x32_bf16 v[128:131], a[160:163], a[64:67], 0\n"
  "s_waitcnt lgkmcnt(14)\n"
  "ds_read_b128 a[160:163], v212 offset:0\n"
  "s_add_u32 m0, s93, 0\n"
  "v_mfma_f32_16x16x32_bf16 v[128:131], a[164:167], a[68:71], v[128:131]\n"
  "s_waitcnt lgkmcnt(14)\n"
  "ds_read_b128 a[164:167], v213 offset:0\n"
  "buffer_load_dwordx4 v190, s[48:51], 0 offen offset:0 nt lds\n"
  "v_mfma_f32_16x16x32_bf16 v[128:131], a[168:171], a[72:75], v[128:131]\n"
  "s_waitcnt lgkmcnt(14)\n"
  "ds_read_b128 a[168:171], v214 offset:0\n"
  "v_mfma_f32_16x16x32_bf16 v[128:131], a[172:175], a[76:79], v[128:131]\n"
  "s_waitcnt lgkmcnt(14)\n"
  "ds_read_b128 a[172:175], v215 offset:0\n"
  "v_mfma_f32_16x16x32_bf16 v[128:131], a[176:179], a[80:83], v[128:131]\n"
  "s_waitcnt lgkmcnt(14)\n"
  "ds_read_b128 a[176:179], v212 offset:256\n"
  "v_mfma_f32_16x16x32_bf16 v[128:131], a[180:183], a[84:87], v[128:131]\n"
  "s_waitcnt lgkmcnt(14)\n"
  "ds_read_b128 a[180:183], v213 offset:256\n"
  "v_mfma_f32_16x16x32_bf16 v[128:131], a[184:187], a[88:91], v[128:131]\n"
  "s_waitcnt lgkmcnt(14)\n"
  "ds_read_b128 a[184:187], v214 offset:256\n"
  "v_mfma_f32_16x16x32_bf16 v[128:131], a[188:191], a[92:95], v[128:131]\n"
  "s_waitcnt lgkmcnt(14)\n"
  "ds_read_b128 a[188:191], v215 offset:256\n"
  "v_mfma_f32_16x16x32_bf16 v[168:171], a[192:195], a[64:67], 0\n"
  "s_waitcnt lgkmcnt(8)\n"
  "ds_read_b128 a[192:195], v212 offset:1024\n"
  "s_add_u32 m0, s93, 1024\n"
  "v_mfma_f32_16x16x32_bf16 v[168:171], a[196:199], a[68:71], v[168:171]\n"
  "ds_read_b128 a[196:199], v213 offset:1024\n"
  "buffer_load_dwordx4 v190, s[48:51], s82 offen offset:0 nt lds\n"
  "v_mfma_f32_16x16x32_bf16 v[168:171], a[200:203], a[72:75], v[168:171]\n"
  "ds_read_b128 a[200:203], v214 offset:1024\n"
  "v_mfma_f32_16x16x32_bf16 v[168:171], a[204:207], a[76:79], v[168:171]\n"
  "ds_read_b128 a[204:207], v215 offset:1024\n"
  "v_mfma_f32_16x16x32_bf16 v[168:171], a[208:211], a[80:83], v[168:171]\n"
  "ds_read_b128 a[208:211], v212 offset:1280\n"
  "v_max_f32 v244, v128, v129\n"
  "v_max3_f32 v244, v130, v131, v244\n"
  "v_mfma_f32_16x16x32_bf16 v[168:171], a[212:215], a[84:87], v[168:171]\n"
  "ds_read_b128 a[212:215], v213 offset:1280\n"
  "v_mov_b32 v247, v244\n"
  "v_mfma_f32_16x16x32_bf16 v[168:171], a[216:219], a[88:91], v[168:171]\n"
  "ds_read_b128 a[216:219], v214 offset:1280\n"
  "v_permlane32_swap_b32 v244, v247\n"
  "v_max_f32 v244, v244, v247\n"
  "v_mfma_f32_16x16x32_bf16 v[168:171], a[220:223], a[92:95], v[168:171]\n"
  "s_waitcnt lgkmcnt(8)\n"
  "ds_read_b128 a[220:223], v215 offset:1280\n"
  "v_mov_b32 v247, v244\n"
  "v_mfma_f32_16x16x32_bf16 v[140:143], a[160:163], a[64:67], 0\n"
  "ds_read_b128 a[160:163], v208 offset:0\n"
  "v_permlane16_swap_b32 v244, v247\n"
  "v_max_f32 v244, v244, v247\n"
  "s_add_u32 m0, s93, 1920\n"
  "v_mfma_f32_16x16x32_bf16 v[140:143], a[164:167], a[68:71], v[140:143]\n"
  "ds_read_b128 a[164:167], v209 offset:0\n"
  "buffer_load_dwordx4 v190, s[48:51], 0 offen offset:128 nt lds\n"
  "v_mfma_f32_16x16x32_bf16 v[140:143], a[168:171], a[72:75], v[140:143]\n"
  "ds_read_b128 a[168:171], v210 offset:0\n"
  "v_mul_f32 v244, s56, v244\n"
  "v_ceil_f32 v232, v244\n"
  "v_mfma_f32_16x16x32_bf16 v[140:143], a[172:175], a[76:79], v[140:143]\n"
  "ds_read_b128 a[172:175], v211 offset:0\n"
  "v_fma_f32 v128, v128, s56, -v232\n"
  "v_fma_f32 v129, v129, s56, -v232\n"
  "v_mfma_f32_16x16x32_bf16 v[140:143], a[176:179], a[80:83], v[140:143]\n"
  "ds_read_b128 a[176:179], v208 offset:256\n"
  "v_fma_f32 v130, v130, s56, -v232\n"
  "v_fma_f32 v131, v131, s56, -v232\n"
  "v_mfma_f32_16x16x32_bf16 v[140:143], a[180:183], a[84:87], v[140:143]\n"
  "ds_read_b128 a[180:183], v209 offset:256\n"
  "v_exp_f32 v128, v128\n"
  "v_mfma_f32_16x16x32_bf16 v[140:143], a[184:187], a[88:91], v[140:143]\n"
  "s_waitcnt lgkmcnt(8)\n"
  "ds_read_b128 a[184:187], v210 offset:256\n"
  "v_exp_f32 v129, v129\n"
  "v_mfma_f32_16x16x32_bf16 v[140:143], a[188:191], a[92:95], v[140:143]\n"
  "ds_read_b128 a[188:191], v211 offset:256\n"
  "v_exp_f32 v130, v130\n"
  "v_mfma_f32_16x16x32_bf16 v[180:183], a[192:195], a[64:67], 0\n"
  "ds_read_b128 a[192:195], v208 offset:1024\n"
  "v_exp_f32 v131, v131\n"
  "s_add_u32 m0, s93, 2944\n"
  "v_mfma_f32_16x16x32_bf16 v[180:183], a[196:199], a[68:71], v[180:183]\n"
  "ds_read_b128 a[196:199], v209 offset:1024\n"
  "buffer_load_dwordx4 v190, s[48:51], s82 offen offset:128 nt lds\n"
  "v_mfma_f32_16x16x32_bf16 v[180:183], a[200:203], a[72:75], v[180:183]\n"
  "ds_read_b128 a[200:203], v210 offset:1024\n"
  "v_add_f32 v238, v128, v129\n"
  "v_add_f32 v238, v238, v130\n"
  "v_mfma_f32_16x16x32_bf16 v[180:183], a[204:207], a[76:79], v[180:183]\n"
  "ds_read_b128 a[204:207], v211 offset:1024\n"
  "v_add_f32 v238, v238, v131\n"
  "v_cvt_pk_bf16_f32 v128, v128, v129\n"
  "v_mfma_f32_16x16x32_bf16 v[180:183], a[208:211], a[80:83], v[180:183]\n"
  "ds_read_b128 a[208:211], v208 offset:1280\n"
  "v_cvt_pk_bf16_f32 v129, v130, v131\n"
  "v_fma_f32 v168, v168, s56, -v232\n"
  "v_mfma_f32_16x16x32_bf16 v[180:183], a[212:215], a[84:87], v[180:183]\n"
  "s_waitcnt lgkmcnt(8)\n"
  "ds_read_b128 a[212:215], v209 offset:1280\n"
  "v_fma_f32 v169, v169, s56, -v232\n"
  "v_fma_f32 v170, v170, s56, -v232\n"
  "v_mfma_f32_16x16x32_bf16 v[180:183], a[216:219], a[88:91], v[180:183]\n"
  "ds_read_b128 a[216:219], v210 offset:1280\n"
  "v_fma_f32 v171, v171, s56, -v232\n"
  "v_mfma_f32_16x16x32_bf16 v[180:183], a[220:223], a[92:95], v[180:183]\n"
  "ds_read_b128 a[220:223], v211 offset:1280\n"
  "v_exp_f32 v168, v168\n"
  "s_add_u32 s48, s48, 319488\n"
  "s_addc_u32 s49, s49, 0\n"
  "v_exp_f32 v169, v169\n"
  "v_exp_f32 v170, v170\n"
  "v_exp_f32 v171, v171\n"
  "v_add_f32 v238, v238, v168\n"
  "v_add_f32 v238, v238, v169\n"
  "v_add_f32 v238, v238, v170\n"
  "v_add_f32 v238, v238, v171\n"
  "v_cvt_pk_bf16_f32 v130, v168, v169\n"
  "v_cvt_pk_bf16_f32 v131, v170, v171\n"
  "v_fma_f32 v140, v140, s56, -v232\n"
  "v_fma_f32 v141, v141, s56, -v232\n"
  "v_fma_f32 v142, v142, s56, -v232\n"
  "v_fma_f32 v143, v143, s56, -v232\n"
  "v_exp_f32 v140, v140\n"
  "v_exp_f32 v141, v141\n"
  "v_exp_f32 v142, v142\n"
  "v_exp_f32 v143, v143\n"
  "v_add_f32 v238, v238, v140\n"
  "v_add_f32 v238, v238, v141\n"
  "v_add_f32 v238, v238, v142\n"
  "v_add_f32 v238, v238, v143\n"
  "v_cvt_pk_bf16_f32 v140, v140, v141\n"
  "v_cvt_pk_bf16_f32 v141, v142, v143\n"
  "v_fma_f32 v180, v180, s56, -v232\n"
  "v_fma_f32 v181, v181, s56, -v232\n"
  "v_fma_f32 v182, v182, s56, -v232\n"
  "v_fma_f32 v183, v183, s56, -v232\n"
  "v_exp_f32 v180, v180\n"
  "v_exp_f32 v181, v181\n"
  "v_exp_f32 v182, v182\n"
  "v_exp_f32 v183, v183\n"
  "v_add_f32 v238, v238, v180\n"
  "v_add_f32 v238, v238, v181\n"
  "v_add_f32 v238, v238, v182\n"
  "v_add_f32 v238, v238, v183\n"
  "s_add_u32 m0, s61, 32768\n"
  "s_nop 0\n"
  "buffer_load_dwordx4 v242, s[40:43], s59 offen offset:0 nt lds\n"
  "buffer_load_dwordx4 v242, s[40:43], s59 offen offset:1024 nt lds\n"
  "buffer_load_dwordx4 v242, s[40:43], s59 offen offset:2048 nt lds\n"
  "buffer_load_dwordx4 v242, s[40:43], s59 offen offset:3072 nt lds\n"
  "s_add_u32 s59, s59, 4096\n"
  "s_add_u32 m0, s61, 36864\n"
  "s_nop 0\n"
  "buffer_load_dwordx4 v242, s[40:43], s59 offen offset:0 nt lds\n"
  "buffer_load_dwordx4 v242, s[40:43], s59 offen offset:1024 nt lds\n"
  "buffer_load_dwordx4 v242, s[40:43], s59 offen offset:2048 nt lds\n"
  "buffer_load_dwordx4 v242, s[40:43], s59 offen offset:3072 nt lds\n"
  "s_add_u32 s59, s59, 28672\n"
  "s_and_b32 s59, s59, 0x7ffff\n"
  "s_or_b32 s59, s59, s78\n"
  "s_waitcnt vmcnt(12) lgkmcnt(0)\n"
  "s_barrier\n"
  "ds_read_b128 a[96:99], v152 offset:32768\n"
  "ds_read_b128 a[100:103], v153 offset:32768\n"
  "ds_read_b128 a[104:107], v154 offset:32768\n"
  "ds_read_b128 a[108:111], v155 offset:32768\n"
  "ds_read_b128 a[112:115], v152 offset:33024\n"
  "ds_read_b128 a[116:119], v153 offset:33024\n"
  "ds_read_b128 a[120:123], v154 offset:33024\n"
  "ds_read_b128 a[124:127], v155 offset:33024\n"
  "s_waitcnt lgkmcnt(7)\n"
  "v_mfma_f32_16x16x32_bf16 v[132:135], a[160:163], a[96:99], 0\n"
  "s_waitcnt lgkmcnt(6)\n"
  "ds_read_b128 a[160:163], v212 offset:0\n"
  "s_add_u32 m0, s61, 65536\n"
  "v_mfma_f32_16x16x32_bf16 v[132:135], a[164:167], a[100:103], v[132:135]\n"
  "s_waitcnt lgkmcnt(6)\n"
  "ds_read_b128 a[164:167], v213 offset:0\n"
  "buffer_load_dwordx4 v242, s[44:47], s60 offen offset:0 lds\n"
  "v_mfma_f32_16x16x32_bf16 v[132:135], a[168:171], a[104:107], v[132:135]\n"
  "s_waitcnt lgkmcnt(6)\n"
  "ds_read_b128 a[168:171], v214 offset:0\n"
  "v_mfma_f32_16x16x32_bf16 v[132:135], a[172:175], a[108:111], v[132:135]\n"
  "s_waitcnt lgkmcnt(6)\n"
  "ds_read_b128 a[172:175], v215 offset:0\n"
  "v_mfma_f32_16x16x32_bf16 v[132:135], a[176:179], a[112:115], v[132:135]\n"
  "s_waitcnt lgkmcnt(6)\n"
  "ds_read_b128 a[176:179], v212 offset:256\n"
  "v_mfma_f32_16x16x32_bf16 v[132:135], a[180:183], a[116:119], v[132:135]\n"
  "s_waitcnt lgkmcnt(6)\n"
  "ds_read_b128 a[180:183], v213 offset:256\n"
  "buffer_load_dwordx4 v242, s[44:47], s60 offen offset:1024 lds\n"
  "v_mfma_f32_16x16x32_bf16 v[132:135], a[184:187], a[120:123], v[132:135]\n"
  "s_waitcnt lgkmcnt(6)\n"
  "ds_read_b128 a[184:187], v214 offset:256\n"
  "v_mfma_f32_16x16x32_bf16 v[132:135], a[188:191], a[124:127], v[132:135]\n"
  "ds_read_b128 a[188:191], v215 offset:256\n"
  "v_mfma_f32_16x16x32_bf16 v[172:175], a[192:195], a[96:99], 0\n"
  "ds_read_b128 a[192:195], v212 offset:1024\n"
  "v_mfma_f32_16x16x32_bf16 v[172:175], a[196:199], a[100:103], v[172:175]\n"
  "ds_read_b128 a[196:199], v213 offset:1024\n"
  "buffer_load_dwordx4 v242, s[44:47], s60 offen offset:2048 lds\n"
  "v_mfma_f32_16x16x32_bf16 v[172:175], a[200:203], a[104:107], v[172:175]\n"
  "ds_read_b128 a[200:203], v214 offset:1024\n"
  "v_mfma_f32_16x16x32_bf16 v[172:175], a[204:207], a[108:111], v[172:175]\n"
  "ds_read_b128 a[204:207], v215 offset:1024\n"
  "v_mfma_f32_16x16x32_bf16 v[172:175], a[208:211], a[112:115], v[172:175]\n"
  "ds_read_b128 a[208:211], v212 offset:1280\n"
  "v_max_f32 v245, v132, v133\n"
  "v_max3_f32 v245, v134, v135, v245\n"
  "v_mfma_f32_16x16x32_bf16 v[172:175], a[212:215], a[116:119], v[172:175]\n"
  "ds_read_b128 a[212:215], v213 offset:1280\n"
  "buffer_load_dwordx4 v242, s[44:47], s60 offen offset:3072 lds\n"
  "v_mfma_f32_16x16x32_bf16 v[172:175], a[216:219], a[120:123], v[172:175]\n"
  "ds_read_b128 a[216:219], v214 offset:1280\n"
  "v_mov_b32 v248, v245\n"
  "v_mfma_f32_16x16x32_bf16 v[172:175], a[220:223], a[124:127], v[172:175]\n"
  "s_waitcnt lgkmcnt(8)\n"
  "ds_read_b128 a[220:223], v215 offset:1280\n"
  "v_permlane32_swap_b32 v245, v248\n"
  "v_max_f32 v245, v245, v248\n"
  "v_mfma_f32_16x16x32_bf16 v[144:147], a[160:163], a[96:99], 0\n"
  "ds_read_b128 a[160:163], v208 offset:0\n"
  "v_mov_b32 v248, v245\n"
  "s_add_u32 s60, s60, 4096\n"
  "s_add_u32 m0, s61, 69632\n"
  "v_mfma_f32_16x16x32_bf16 v[144:147], a[164:167], a[100:103], v[144:147]\n"
  "ds_read_b128 a[164:167], v209 offset:0\n"
  "buffer_load_dwordx4 v242, s[44:47], s60 offen offset:0 lds\n"
  "v_mfma_f32_16x16x32_bf16 v[144:147], a[168:171], a[104:107], v[144:147]\n"
  "ds_read_b128 a[168:171], v210 offset:0\n"
  "v_permlane16_swap_b32 v245, v248\n"
  "v_max_f32 v245, v245, v248\n"
  "v_mfma_f32_16x16x32_bf16 v[144:147], a[172:175], a[108:111], v[144:147]\n"
  "ds_read_b128 a[172:175], v211 offset:0\n"
  "v_mul_f32 v245, s56, v245\n"
  "v_ceil_f32 v233, v245\n"
  "v_mfma_f32_16x16x32_bf16 v[144:147], a[176:179], a[112:115], v[144:147]\n"
  "ds_read_b128 a[176:179], v208 offset:256\n"
  "v_fma_f32 v132, v132, s56, -v233\n"
  "v_fma_f32 v133, v133, s56, -v233\n"
  "v_mfma_f32_16x16x32_bf16 v[144:147], a[180:183], a[116:119], v[144:147]\n"
  "ds_read_b128 a[180:183], v209 offset:256\n"
  "buffer_load_dwordx4 v242, s[44:47], s60 offen offset:1024 lds\n"
  "v_mfma_f32_16x16x32_bf16 v[144:147], a[184:187], a[120:123], v[144:147]\n"
  "s_waitcnt lgkmcnt(8)\n"
  "ds_read_b128 a[184:187], v210 offset:256\n"
  "v_fma_f32 v134, v134, s56, -v233\n"
  "v_fma_f32 v135, v135, s56, -v233\n"
  "v_mfma_f32_16x16x32_bf16 v[144:147], a[188:191], a[124:127], v[144:147]\n"
  "ds_read_b128 a[188:191], v211 offset:256\n"
  "v_exp_f32 v132, v132\n"
  "v_mfma_f32_16x16x32_bf16 v[184:187], a[192:195], a[96:99], 0\n"
  "ds_read_b128 a[192:195], v208 offset:1024\n"
  "v_exp_f32 v133, v133\n"
  "v_mfma_f32_16x16x32_bf16 v[184:187], a[196:199], a[100:103], v[184:187]\n"
  "ds_read_b128 a[196:199], v209 offset:1024\n"
  "buffer_load_dwordx4 v242, s[44:47], s60 offen offset:2048 lds\n"
  "v_mfma_f32_16x16x32_bf16 v[184:187], a[200:203], a[104:107], v[184:187]\n"
  "ds_read_b128 a[200:203], v210 offset:1024\n"
  "v_exp_f32 v134, v134\n"
  "v_mfma_f32_16x16x32_bf16 v[184:187], a[204:207], a[108:111], v[184:187]\n"
  "ds_read_b128 a[204:207], v211 offset:1024\n"
  "v_exp_f32 v135, v135\n"
  "v_mfma_f32_16x16x32_bf16 v[184:187], a[208:211], a[112:115], v[184:187]\n"
  "ds_read_b128 a[208:211], v208 offset:1280\n"
  "v_add_f32 v239, v132, v133\n"
  "v_add_f32 v239, v239, v134\n"
  "v_mfma_f32_16x16x32_bf16 v[184:187], a[212:215], a[116:119], v[184:187]\n"
  "s_waitcnt lgkmcnt(8)\n"
  "ds_read_b128 a[212:215], v209 offset:1280\n"
  "buffer_load_dwordx4 v242, s[44:47], s60 offen offset:3072 lds\n"
  "v_mfma_f32_16x16x32_bf16 v[184:187], a[216:219], a[120:123], v[184:187]\n"
  "ds_read_b128 a[216:219], v210 offset:1280\n"
  "v_add_f32 v239, v239, v135\n"
  "v_cvt_pk_bf16_f32 v132, v132, v133\n"
  "v_mfma_f32_16x16x32_bf16 v[184:187], a[220:223], a[124:127], v[184:187]\n"
  "ds_read_b128 a[220:223], v211 offset:1280\n"
  "v_cvt_pk_bf16_f32 v133, v134, v135\n"
  "v_fma_f32 v172, v172, s56, -v233\n"
  "s_add_u32 s60, s60, 28672\n"
  "s_and_b32 s60, s60, 0x7ffff\n"
  "s_or_b32 s60, s60, s78\n"
  "v_fma_f32 v173, v173, s56, -v233\n"
  "v_fma_f32 v174, v174, s56, -v233\n"
  "v_fma_f32 v175, v175, s56, -v233\n"
  "v_exp_f32 v172, v172\n"
  "v_exp_f32 v173, v173\n"
  "v_exp_f32 v174, v174\n"
  "v_exp_f32 v175, v175\n"
  "v_add_f32 v239, v239, v172\n"
  "v_add_f32 v239, v239, v173\n"
  "v_add_f32 v239, v239, v174\n"
  "v_add_f32 v239, v239, v175\n"
  "v_cvt_pk_bf16_f32 v134, v172, v173\n"
  "v_cvt_pk_bf16_f32 v135, v174, v175\n"
  "v_fma_f32 v144, v144, s56, -v233\n"
  "v_fma_f32 v145, v145, s56, -v233\n"
  "v_fma_f32 v146, v146, s56, -v233\n"
  "v_fma_f32 v147, v147, s56, -v233\n"
  "v_exp_f32 v144, v144\n"
  "v_exp_f32 v145, v145\n"
  "v_exp_f32 v146, v146\n"
  "v_exp_f32 v147, v147\n"
  "v_add_f32 v239, v239, v144\n"
  "v_add_f32 v239, v239, v145\n"
  "v_add_f32 v239, v239, v146\n"
  "s_waitcnt vmcnt(16) lgkmcnt(0)\n"
  "s_barrier\n"
  "ds_read_b128 a[128:131], v197 offset:0\n"
  "ds_read_b128 a[132:135], v198 offset:0\n"
  "ds_read_b128 a[136:139], v197 offset:2048\n"
  "ds_read_b128 a[140:143], v198 offset:2048\n"
  "ds_read_b128 a[144:147], v197 offset:4096\n"
  "ds_read_b128 a[148:151], v198 offset:4096\n"
  "ds_read_b128 a[152:155], v197 offset:6144\n"
  "ds_read_b128 a[156:159], v198 offset:6144\n"
  "s_waitcnt lgkmcnt(7)\n"
  "v_mfma_f32_16x16x32_bf16 v[136:139], a[160:163], a[128:131], 0\n"
  "s_waitcnt lgkmcnt(6)\n"
  "v_add_f32 v239, v239, v147\n"
  "v_fma_f32 v184, v184, s56, -v233\n"
  "v_fma_f32 v185, v185, s56, -v233\n"
  "s_add_u32 m0, s61, 0\n"
  "v_mfma_f32_16x16x32_bf16 v[136:139], a[164:167], a[132:135], v[136:139]\n"
  "s_waitcnt lgkmcnt(5)\n"
  "buffer_load_dwordx4 v242, s[40:43], s59 offen offset:0 nt lds\n"
  "v_fma_f32 v186, v186, s56, -v233\n"
  "v_mfma_f32_16x16x32_bf16 v[136:139], a[168:171], a[136:139], v[136:139]\n"
  "s_waitcnt lgkmcnt(4)\n"
  "v_fma_f32 v187, v187, s56, -v233\n"
  "v_exp_f32 v184, v184\n"
  "v_mfma_f32_16x16x32_bf16 v[136:139], a[172:175], a[140:143], v[136:139]\n"
  "s_waitcnt lgkmcnt(3)\n"
  "v_exp_f32 v185, v185\n"
  "v_mfma_f32_16x16x32_bf16 v[136:139], a[176:179], a[144:147], v[136:139]\n"
  "s_waitcnt lgkmcnt(2)\n"
  "v_exp_f32 v186, v186\n"
  "v_mfma_f32_16x16x32_bf16 v[136:139], a[180:183], a[148:151], v[136:139]\n"
  "s_waitcnt lgkmcnt(1)\n"
  "buffer_load_dwordx4 v242, s[40:43], s59 offen offset:1024 nt lds\n"
  "v_mfma_f32_16x16x32_bf16 v[136:139], a[184:187], a[152:155], v[136:139]\n"
  "s_waitcnt lgkmcnt(0)\n"
  "v_exp_f32 v187, v187\n"
  "v_add_f32 v239, v239, v184\n"
  "v_mfma_f32_16x16x32_bf16 v[136:139], a[188:191], a[156:159], v[136:139]\n"
  "v_add_f32 v239, v239, v185\n"
  "v_add_f32 v239, v239, v186\n"
  "v_add_f32 v239, v239, v187\n"
  "v_mfma_f32_16x16x32_bf16 v[176:179], a[192:195], a[128:131], 0\n"
  "v_mfma_f32_16x16x32_bf16 v[176:179], a[196:199], a[132:135], v[176:179]\n"
  "buffer_load_dwordx4 v242, s[40:43], s59 offen offset:2048 nt lds\n"
  "v_mfma_f32_16x16x32_bf16 v[176:179], a[200:203], a[136:139], v[176:179]\n"
  "v_mfma_f32_16x16x32_bf16 v[176:179], a[204:207], a[140:143], v[176:179]\n"
  "v_mfma_f32_16x16x32_bf16 v[176:179], a[208:211], a[144:147], v[176:179]\n"
  "v_max_f32 v246, v136, v137\n"
  "v_max3_f32 v246, v138, v139, v246\n"
  "v_mov_b32 v249, v246\n"
  "v_mfma_f32_16x16x32_bf16 v[176:179], a[212:215], a[148:151], v[176:179]\n"
  "buffer_load_dwordx4 v242, s[40:43], s59 offen offset:3072 nt lds\n"
  "v_mfma_f32_16x16x32_bf16 v[176:179], a[216:219], a[152:155], v[176:179]\n"
  "v_permlane32_swap_b32 v246, v249\n"
  "v_max_f32 v246, v246, v249\n"
  "v_mov_b32 v249, v246\n"
  "v_mfma_f32_16x16x32_bf16 v[176:179], a[220:223], a[156:159], v[176:179]\n"
  "s_nop 0\n"
  "v_permlane16_swap_b32 v246, v249\n"
  "v_max_f32 v246, v246, v249\n"
  "v_mul_f32 v246, s56, v246\n"
  "v_ceil_f32 v234, v246\n"
  "v_add_f32 v234, s89, v234\n"
  "v_fma_f32 v136, v136, s56, -v234\n"
  "v_fma_f32 v137, v137, s56, -v234\n"
  "v_fma_f32 v138, v138, s56, -v234\n"
  "v_fma_f32 v139, v139, s56, -v234\n"
  "v_exp_f32 v136, v136\n"
  "v_exp_f32 v137, v137\n"
  "v_exp_f32 v138, v138\n"
  "s_waitcnt vmcnt(12)\n"
  "s_barrier\n"
  "ds_read_b128 a[160:163], v208 offset:32768\n"
  "ds_read_b128 a[164:167], v209 offset:32768\n"
  "ds_read_b128 a[168:171], v210 offset:32768\n"
  "ds_read_b128 a[172:175], v211 offset:32768\n"
  "ds_read_b128 a[176:179], v208 offset:33024\n"
  "ds_read_b128 a[180:183], v209 offset:33024\n"
  "ds_read_b128 a[184:187], v210 offset:33024\n"
  "ds_read_b128 a[188:191], v211 offset:33024\n"
  "s_add_u32 s59, s59, 4096\n"
  "s_add_u32 m0, s61, 4096\n"
  "s_nop 0\n"
  "buffer_load_dwordx4 v242, s[40:43], s59 offen offset:0 nt lds\n"
  "v_exp_f32 v139, v139\n"
  "v_add_f32 v240, v136, v137\n"
  "v_add_f32 v240, v240, v138\n"
  "buffer_load_dwordx4 v242, s[40:43], s59 offen offset:1024 nt lds\n"
  "v_add_f32 v240, v240, v139\n"
  "v_cvt_pk_bf16_f32 v136, v136, v137\n"
  "v_cvt_pk_bf16_f32 v137, v138, v139\n"
  "buffer_load_dwordx4 v242, s[40:43], s59 offen offset:2048 nt lds\n"
  "v_fma_f32 v176, v176, s56, -v234\n"
  "v_fma_f32 v177, v177, s56, -v234\n"
  "v_fma_f32 v178, v178, s56, -v234\n"
  "buffer_load_dwordx4 v242, s[40:43], s59 offen offset:3072 nt lds\n"
  "v_fma_f32 v179, v179, s56, -v234\n"
  "v_exp_f32 v176, v176\n"
  "v_exp_f32 v177, v177\n"
  "s_add_u32 s59, s59, 28672\n"
  "s_and_b32 s59, s59, 0x7ffff\n"
  "s_or_b32 s59, s59, s78\n"
  "buffer_load_dword v254, v253, s[96:99], s68 offen\n"
  "v_exp_f32 v178, v178\n"
  "v_exp_f32 v179, v179\n"
  "v_add_f32 v240, v240, v176\n"
  "s_add_u32 s68, s68, 8192\n"
  "v_add_f32 v240, v240, v177\n"
  "v_add_f32 v240, v240, v178\n"
  "v_add_f32 v240, v240, v179\n"
  "v_cvt_pk_bf16_f32 v138, v176, v177\n"
  "v_cvt_pk_bf16_f32 v139, v178, v179\n"
  "v_max3_f32 v243, v238, v239, v240\n"
  "v_cmp_nge_f32 vcc, s57, v243\n"
  "s_add_u32 s58, s58, 1\n"
  "s_waitcnt lgkmcnt(0)\n"
  "s_cbranch_vccz .Lattn_LOOP\n"
  "s_branch .Lattn_RARE0\n"
  ".Lattn_BLOCK:\n"
  "v_mfma_f32_16x16x32_bf16 v[128:131], a[160:163], a[64:67], 0\n"
  "s_add_u32 m0, s61, 65536\n"
  "ds_read_b128 a[220:223], v211 offset:1280\n"
  "v_mov_b32 v148, 0\n"
  "v_mfma_f32_16x16x32_bf16 v[128:131], a[164:167], a[68:71], v[128:131]\n"
  "buffer_load_dwordx4 v242, s[44:47], s60 offen offset:0 lds\n"
  "v_mfma_f32_16x16x32_bf16 v[128:131], a[168:171], a[72:75], v[128:131]\n"
  "ds_read_b128 a[216:219], v210 offset:1280\n"
  "v_mov_b32 v149, 0\n"
  "v_mfma_f32_16x16x32_bf16 v[128:131], a[172:175], a[76:79], v[128:131]\n"
  "ds_read_b128 a[212:215], v209 offset:1280\n"
  "v_mov_b32 v150, 0\n"
  "v_mfma_f32_16x16x32_bf16 v[128:131], a[176:179], a[80:83], v[128:131]\n"
  "buffer_load_dwordx4 v242, s[44:47], s60 offen offset:1024 lds\n"
  "v_mfma_f32_16x16x32_bf16 v[128:131], a[180:183], a[84:87], v[128:131]\n"
  "ds_read_b128 a[208:211], v208 offset:1280\n"
  "ds_read_b128 a[204:207], v211 offset:1024\n"
  "v_mfma_f32_16x16x32_bf16 v[128:131], a[184:187], a[88:91], v[128:131]\n"
  "v_mov_b32 v151, 0\n"
  "v_mov_b32 v235, 0\n"
  "v_mfma_f32_16x16x32_bf16 v[128:131], a[188:191], a[92:95], v[128:131]\n"
  "buffer_load_dwordx4 v242, s[44:47], s60 offen offset:2048 lds\n"
  "v_mfma_f32_16x16x32_bf16 v[132:135], a[188:191], a[124:127], 0\n"
  "ds_read_b128 a[200:203], v210 offset:1024\n"
  "ds_read_b128 a[196:199], v209 offset:1024\n"
  "v_mfma_f32_16x16x32_bf16 v[132:135], a[184:187], a[120:123], v[132:135]\n"
  "ds_read_b128 a[192:195], v208 offset:1024\n"
  "v_mov_b32 v236, 0\n"
  "v_mfma_f32_16x16x32_bf16 v[132:135], a[180:183], a[116:119], v[132:135]\n"
  "buffer_load_dwordx4 v242, s[44:47], s60 offen offset:3072 lds\n"
  "v_mfma_f32_16x16x32_bf16 v[132:135], a[176:179], a[112:115], v[132:135]\n"
  "s_add_u32 s60, s60, 4096\n"
  "s_add_u32 m0, s61, 69632\n"
  "v_mov_b32 v237, 0\n"
  "v_mfma_f32_16x16x32_bf16 v[132:135], a[172:175], a[108:111], v[132:135]\n"
  "buffer_load_dwordx4 v242, s[44:47], s60 offen offset:0 lds\n"
  "v_mfma_f32_16x16x32_bf16 v[132:135], a[168:171], a[104:107], v[132:135]\n"
  "v_max_f32 v244, v128, v129\n"
  "v_max3_f32 v244, v130, v131, v244\n"
  "v_mfma_f32_16x16x32_bf16 v[132:135], a[164:167], a[100:103], v[132:135]\n"
  "v_mov_b32 v247, v244\n"
  "v_mfma_f32_16x16x32_bf16 v[132:135], a[160:163], a[96:99], v[132:135]\n"
  "s_nop 0\n"
  "v_permlane32_swap_b32 v244, v247\n"
  "v_mfma_f32_16x16x32_bf16 v[136:139], a[160:163], a[128:131], 0\n"
  "v_max_f32 v244, v244, v247\n"
  "v_mov_b32 v247, v244\n"
  "v_mfma_f32_16x16x32_bf16 v[136:139], a[164:167], a[132:135], v[136:139]\n"
  "s_nop 0\n"
  "v_permlane16_swap_b32 v244, v247\n"
  "v_mfma_f32_16x16x32_bf16 v[136:139], a[168:171], a[136:139], v[136:139]\n"
  "v_max_f32 v244, v244, v247\n"
  "v_mul_f32 v244, s56, v244\n"
  "v_mfma_f32_16x16x32_bf16 v[136:139], a[172:175], a[140:143], v[136:139]\n"
  "v_ceil_f32 v232, v244\n"
  "v_fma_f32 v128, v128, s56, -v232\n"
  "v_mfma_f32_16x16x32_bf16 v[136:139], a[176:179], a[144:147], v[136:139]\n"
  "buffer_load_dwordx4 v242, s[44:47], s60 offen offset:1024 lds\n"
  "v_mfma_f32_16x16x32_bf16 v[136:139], a[180:183], a[148:151], v[136:139]\n"
  "v_max_f32 v245, v132, v133\n"
  "v_max3_f32 v245, v134, v135, v245\n"
  "v_mfma_f32_16x16x32_bf16 v[136:139], a[184:187], a[152:155], v[136:139]\n"
  "ds_read_b128 a[160:163], v212 offset:0\n"
  "ds_read_b128 a[164:167], v213 offset:0\n"
  "v_mfma_f32_16x16x32_bf16 v[136:139], a[188:191], a[156:159], v[136:139]\n"
  "s_waitcnt lgkmcnt(2)\n"
  "v_mov_b32 v248, v245\n"
  "v_fma_f32 v129, v129, s56, -v232\n"
  "v_mfma_f32_16x16x32_bf16 v[176:179], a[220:223], a[156:159], 0\n"
  "v_permlane32_swap_b32 v245, v248\n"
  "v_mfma_f32_16x16x32_bf16 v[176:179], a[216:219], a[152:155], v[176:179]\n"
  "v_max_f32 v245, v245, v248\n"
  "v_mov_b32 v248, v245\n"
  "v_mfma_f32_16x16x32_bf16 v[176:179], a[212:215], a[148:151], v[176:179]\n"
  "ds_read_b128 a[168:171], v214 offset:0\n"
  "v_fma_f32 v130, v130, s56, -v232\n"
  "v_mfma_f32_16x16x32_bf16 v[176:179], a[208:211], a[144:147], v[176:179]\n"
  "v_permlane16_swap_b32 v245, v248\n"
  "v_mfma_f32_16x16x32_bf16 v[176:179], a[204:207], a[140:143], v[176:179]\n"
  "buffer_load_dwordx4 v242, s[44:47], s60 offen offset:2048 lds\n"
  "v_mfma_f32_16x16x32_bf16 v[176:179], a[200:203], a[136:139], v[176:179]\n"
  "ds_read_b128 a[172:175], v215 offset:0\n"
  "v_max_f32 v245, v245, v248\n"
  "v_mfma_f32_16x16x32_bf16 v[176:179], a[196:199], a[132:135], v[176:179]\n"
  "ds_read_b128 a[176:179], v212 offset:256\n"
  "v_mul_f32 v245, s56, v245\n"
  "v_mfma_f32_16x16x32_bf16 v[176:179], a[192:195], a[128:131], v[176:179]\n"
  "v_ceil_f32 v233, v245\n"
  "v_max_f32 v246, v136, v137\n"
  "v_mfma_f32_16x16x32_bf16 v[172:175], a[192:195], a[96:99], 0\n"
  "v_max3_f32 v246, v138, v139, v246\n"
  "v_mov_b32 v249, v246\n"
  "v_mfma_f32_16x16x32_bf16 v[172:175], a[196:199], a[100:103], v[172:175]\n"
  "ds_read_b128 a[180:183], v213 offset:256\n"
  "v_fma_f32 v131, v131, s56, -v232\n"
  "v_mfma_f32_16x16x32_bf16 v[172:175], a[200:203], a[104:107], v[172:175]\n"
  "ds_read_b128 a[184:187], v214 offset:256\n"
  "v_fma_f32 v132, v132, s56, -v233\n"
  "v_mfma_f32_16x16x32_bf16 v[172:175], a[204:207], a[108:111], v[172:175]\n"
  "v_permlane32_swap_b32 v246, v249\n"
  "v_mfma_f32_16x16x32_bf16 v[172:175], a[208:211], a[112:115], v[172:175]\n"
  "buffer_load_dwordx4 v242, s[44:47], s60 offen offset:3072 lds\n"
  "v_mfma_f32_16x16x32_bf16 v[172:175], a[212:215], a[116:119], v[172:175]\n"
  "ds_read_b128 a[188:191], v215 offset:256\n"
  "v_max_f32 v246, v246, v249\n"
  "v_mfma_f32_16x16x32_bf16 v[172:175], a[216:219], a[120:123], v[172:175]\n"
  "v_mov_b32 v249, v246\n"
  "v_fma_f32 v133, v133, s56, -v233\n"
  "v_mfma_f32_16x16x32_bf16 v[172:175], a[220:223], a[124:127], v[172:175]\n"
  "v_permlane16_swap_b32 v246, v249\n"
  "v_mfma_f32_16x16x32_bf16 v[168:171], a[220:223], a[92:95], 0\n"
  "s_add_u32 s60, s60, 28672\n"
  "s_and_b32 s60, s60, 0x7ffff\n"
  "s_or_b32 s60, s60, s78\n"
  "v_max_f32 v246, v246, v249\n"
  "v_mul_f32 v246, s56, v246\n"
  "v_mfma_f32_16x16x32_bf16 v[168:171], a[216:219], a[88:91], v[168:171]\n"
  "ds_read_b128 a[216:219], v214 offset:1280\n"
  "ds_read_b128 a[220:223], v215 offset:1280\n"
  "v_mfma_f32_16x16x32_bf16 v[168:171], a[212:215], a[84:87], v[168:171]\n"
  "ds_read_b128 a[212:215], v213 offset:1280\n"
  "v_ceil_f32 v234, v246\n"
  "v_mfma_f32_16x16x32_bf16 v[168:171], a[208:211], a[80:83], v[168:171]\n"
  "ds_read_b128 a[208:211], v212 offset:1280\n"
  "v_add_f32 v234, s89, v234\n"
  "v_mfma_f32_16x16x32_bf16 v[168:171], a[204:207], a[76:79], v[168:171]\n"
  "v_exp_f32 v128, v128\n"
  "v_mfma_f32_16x16x32_bf16 v[168:171], a[200:203], a[72:75], v[168:171]\n"
  "ds_read_b128 a[204:207], v215 offset:1024\n"
  "v_fma_f32 v134, v134, s56, -v233\n"
  "v_mfma_f32_16x16x32_bf16 v[168:171], a[196:199], a[68:71], v[168:171]\n"
  "v_exp_f32 v129, v129\n"
  "v_mfma_f32_16x16x32_bf16 v[168:171], a[192:195], a[64:67], v[168:171]\n"
  "s_waitcnt lgkmcnt(5)\n"
  "ds_read_b128 a[196:199], v213 offset:1024\n"
  "ds_read_b128 a[200:203], v214 offset:1024\n"
  "v_mfma_f32_16x16x32_bf16 v[140:143], a[160:163], a[64:67], 0\n"
  "v_exp_f32 v130, v130\n"
  "v_mfma_f32_16x16x32_bf16 v[140:143], a[164:167], a[68:71], v[140:143]\n"
  "ds_read_b128 a[192:195], v212 offset:1024\n"
  "v_add_f32 v238, v128, v129\n"
  "v_mfma_f32_16x16x32_bf16 v[140:143], a[168:171], a[72:75], v[140:143]\n"
  "s_mov_b32 s57, s88\n"
  "v_exp_f32 v131, v131\n"
  "v_mfma_f32_16x16x32_bf16 v[140:143], a[172:175], a[76:79], v[140:143]\n"
  "v_exp_f32 v132, v132\n"
  "v_mfma_f32_16x16x32_bf16 v[140:143], a[176:179], a[80:83], v[140:143]\n"
  "v_exp_f32 v133, v133\n"
  "v_mfma_f32_16x16x32_bf16 v[140:143], a[180:183], a[84:87], v[140:143]\n"
  "v_exp_f32 v134, v134\n"
  "v_mfma_f32_16x16x32_bf16 v[140:143], a[184:187], a[88:91], v[140:143]\n"
  "v_add_f32 v238, v238, v130\n"
  "v_add_f32 v238, v238, v131\n"
  "v_mfma_f32_16x16x32_bf16 v[140:143], a[188:191], a[92:95], v[140:143]\n"
  "v_cvt_pk_bf16_f32 v128, v128, v129\n"
  "v_cvt_pk_bf16_f32 v129, v130, v131\n"
  "v_mfma_f32_16x16x32_bf16 v[144:147], a[188:191], a[124:127], 0\n"
  "v_fma_f32 v135, v135, s56, -v233\n"
  "v_add_f32 v239, v132, v133\n"
  "v_mfma_f32_16x16x32_bf16 v[144:147], a[184:187], a[120:123], v[144:147]\n"
  "s_waitcnt lgkmcnt(0)\n"
  "v_exp_f32 v135, v135\n"
  "v_mfma_f32_16x16x32_bf16 v[144:147], a[180:183], a[116:119], v[144:147]\n"
  "s_barrier\n"
  "v_add_f32 v239, v239, v134\n"
  "v_add_f32 v239, v239, v135\n"
  "v_mfma_f32_16x16x32_bf16 v[144:147], a[176:179], a[112:115], v[144:147]\n"
  "s_add_u32 m0, s61, 0\n"
  "v_cvt_pk_bf16_f32 v132, v132, v133\n"
  "v_cvt_pk_bf16_f32 v133, v134, v135\n"
  "v_mfma_f32_16x16x32_bf16 v[144:147], a[172:175], a[108:111], v[144:147]\n"
  "buffer_load_dwordx4 v242, s[40:43], s59 offen offset:0 nt lds\n"
  "v_mfma_f32_16x16x32_bf16 v[144:147], a[168:171], a[104:107], v[144:147]\n"
  "v_fma_f32 v136, v136, s56, -v234\n"
  "v_fma_f32 v137, v137, s56, -v234\n"
  "v_mfma_f32_16x16x32_bf16 v[144:147], a[164:167], a[100:103], v[144:147]\n"
  "v_exp_f32 v136, v136\n"
  "v_mfma_f32_16x16x32_bf16 v[144:147], a[160:163], a[96:99], v[144:147]\n"
  "v_exp_f32 v137, v137\n"
  "v_mfma_f32_16x16x32_bf16 v[184:187], a[192:195], a[96:99], 0\n"
  "v_fma_f32 v138, v138, s56, -v234\n"
  "v_fma_f32 v139, v139, s56, -v234\n"
  "v_mfma_f32_16x16x32_bf16 v[184:187], a[196:199], a[100:103], v[184:187]\n"
  "buffer_load_dwordx4 v242, s[40:43], s59 offen offset:1024 nt lds\n"
  "v_mfma_f32_16x16x32_bf16 v[184:187], a[200:203], a[104:107], v[184:187]\n"
  "v_exp_f32 v138, v138\n"
  "v_mfma_f32_16x16x32_bf16 v[184:187], a[204:207], a[108:111], v[184:187]\n"
  "v_exp_f32 v139, v139\n"
  "v_mfma_f32_16x16x32_bf16 v[184:187], a[208:211], a[112:115], v[184:187]\n"
  "v_add_f32 v240, v136, v137\n"
  "v_add_f32 v240, v240, v138\n"
  "v_mfma_f32_16x16x32_bf16 v[184:187], a[212:215], a[116:119], v[184:187]\n"
  "v_add_f32 v240, v240, v139\n"
  "v_cvt_pk_bf16_f32 v136, v136, v137\n"
  "v_mfma_f32_16x16x32_bf16 v[184:187], a[216:219], a[120:123], v[184:187]\n"
  "buffer_load_dwordx4 v242, s[40:43], s59 offen offset:2048 nt lds\n"
  "v_mfma_f32_16x16x32_bf16 v[184:187], a[220:223], a[124:127], v[184:187]\n"
  "v_cvt_pk_bf16_f32 v137, v138, v139\n"
  "v_fma_f32 v176, v176, s56, -v234\n"
  "v_mfma_f32_16x16x32_bf16 v[180:183], a[220:223], a[92:95], 0\n"
  "v_exp_f32 v176, v176\n"
  "v_mfma_f32_16x16x32_bf16 v[180:183], a[216:219], a[88:91], v[180:183]\n"
  "v_fma_f32 v177, v177, s56, -v234\n"
  "v_fma_f32 v178, v178, s56, -v234\n"
  "v_mfma_f32_16x16x32_bf16 v[180:183], a[212:215], a[84:87], v[180:183]\n"
  "v_exp_f32 v177, v177\n"
  "v_mfma_f32_16x16x32_bf16 v[180:183], a[208:211], a[80:83], v[180:183]\n"
  "buffer_load_dwordx4 v242, s[40:43], s59 offen offset:3072 nt lds\n"
  "v_mfma_f32_16x16x32_bf16 v[180:183], a[204:207], a[76:79], v[180:183]\n"
  "v_exp_f32 v178, v178\n"
  "v_mfma_f32_16x16x32_bf16 v[180:183], a[200:203], a[72:75], v[180:183]\n"
  "v_fma_f32 v179, v179, s56, -v234\n"
  "v_add_f32 v240, v240, v176\n"
  "v_mfma_f32_16x16x32_bf16 v[180:183], a[196:199], a[68:71], v[180:183]\n"
  "v_exp_f32 v179, v179\n"
  "v_mfma_f32_16x16x32_bf16 v[180:183], a[192:195], a[64:67], v[180:183]\n"
  "s_add_u32 s59, s59, 4096\n"
  "s_add_u32 m0, s61, 4096\n"
  "v_add_f32 v240, v240, v177\n"
  "v_add_f32 v240, v240, v178\n"
  "v_mfma_f32_32x32x16_bf16 v[0:15], v[148:151], v[148:151], 0\n"
  "buffer_load_dwordx4 v242, s[40:43], s59 offen offset:0 nt lds\n"
  "v_add_f32 v240, v240, v179\n"
  "v_cvt_pk_bf16_f32 v138, v176, v177\n"
  "v_cvt_pk_bf16_f32 v139, v178, v179\n"
  "v_fma_f32 v172, v172, s56, -v233\n"
  "v_mfma_f32_32x32x16_bf16 v[16:31], v[148:151], v[148:151], 0\n"
  "buffer_load_dwordx4 v242, s[40:43], s59 offen offset:1024 nt lds\n"
  "v_fma_f32 v173, v173, s56, -v233\n"
  "v_fma_f32 v174, v174, s56, -v233\n"
  "v_exp_f32 v172, v172\n"
  "v_mfma_f32_32x32x16_bf16 v[32:47], v[148:151], v[148:151], 0\n"
  "buffer_load_dwordx4 v242, s[40:43], s59 offen offset:2048 nt lds\n"
  "v_fma_f32 v175, v175, s56, -v233\n"
  "v_exp_f32 v173, v173\n"
  "v_add_f32 v239, v239, v172\n"
  "v_mfma_f32_32x32x16_bf16 v[48:63], v[148:151], v[148:151], 0\n"
  "buffer_load_dwordx4 v242, s[40:43], s59 offen offset:3072 nt lds\n"
  "v_exp_f32 v174, v174\n"
  "v_exp_f32 v175, v175\n"
  "s_add_u32 s59, s59, 28672\n"
  "s_and_b32 s59, s59, 0x7ffff\n"
  "s_or_b32 s59, s59, s78\n"
  "v_mfma_f32_32x32x16_bf16 v[64:79], v[148:151], v[148:151], 0\n"
  "buffer_load_dword v254, v253, s[96:99], s68 offen\n"
  "v_add_f32 v239, v239, v173\n"
  "v_add_f32 v239, v239, v174\n"
  "v_add_f32 v239, v239, v175\n"
  "v_cvt_pk_bf16_f32 v134, v172, v173\n"
  "v_mfma_f32_32x32x16_bf16 v[80:95], v[148:151], v[148:151], 0\n"
  "v_cvt_pk_bf16_f32 v135, v174, v175\n"
  "v_fma_f32 v168, v168, s56, -v232\n"
  "v_fma_f32 v169, v169, s56, -v232\n"
  "v_fma_f32 v170, v170, s56, -v232\n"
  "v_exp_f32 v168, v168\n"
  "v_mfma_f32_32x32x16_bf16 v[96:111], v[148:151], v[148:151], 0\n"
  "s_waitcnt vmcnt(17)\n"
  "v_fma_f32 v171, v171, s56, -v232\n"
  "v_exp_f32 v169, v169\n"
  "v_exp_f32 v170, v170\n"
  "v_add_f32 v238, v238, v168\n"
  "v_mfma_f32_32x32x16_bf16 v[112:127], v[148:151], v[148:151], 0\n"
  "s_barrier\n"
  "ds_read_b128 a[160:163], v208 offset:32768\n"
  "ds_read_b128 a[164:167], v209 offset:32768\n"
  "v_exp_f32 v171, v171\n"
  "v_add_f32 v238, v238, v169\n"
  "v_add_f32 v238, v238, v170\n"
  "v_mfma_f32_32x32x16_bf16 a[0:15], v[148:151], v[148:151], 0\n"
  "ds_read_b128 a[168:171], v210 offset:32768\n"
  "ds_read_b128 a[172:175], v211 offset:32768\n"
  "v_add_f32 v238, v238, v171\n"
  "v_cvt_pk_bf16_f32 v130, v168, v169\n"
  "v_cvt_pk_bf16_f32 v131, v170, v171\n"
  "v_fma_f32 v140, v140, s56, -v232\n"
  "v_mfma_f32_32x32x16_bf16 a[16:31], v[148:151], v[148:151], 0\n"
  "s_add_u32 s58, s58, 1\n"
  "ds_read_b128 a[176:179], v208 offset:33024\n"
  "ds_read_b128 a[180:183], v209 offset:33024\n"
  "v_fma_f32 v141, v141, s56, -v232\n"
  "v_fma_f32 v142, v142, s56, -v232\n"
  "v_exp_f32 v140, v140\n"
  "v_mfma_f32_32x32x16_bf16 a[32:47], v[148:151], v[148:151], 0\n"
  "ds_read_b128 a[184:187], v210 offset:33024\n"
  "ds_read_b128 a[188:191], v211 offset:33024\n"
  "v_fma_f32 v143, v143, s56, -v232\n"
  "v_exp_f32 v141, v141\n"
  "v_add_f32 v238, v238, v140\n"
  "v_mfma_f32_32x32x16_bf16 a[48:63], v[148:151], v[148:151], 0\n"
  "v_exp_f32 v142, v142\n"
  "v_exp_f32 v143, v143\n"
  "v_add_f32 v238, v238, v141\n"
  "v_add_f32 v238, v238, v142\n"
  "v_add_f32 v238, v238, v143\n"
  "v_cvt_pk_bf16_f32 v140, v140, v141\n"
  "v_cvt_pk_bf16_f32 v141, v142, v143\n"
  "v_fma_f32 v144, v144, s56, -v233\n"
  "v_fma_f32 v145, v145, s56, -v233\n"
  "v_fma_f32 v146, v146, s56, -v233\n"
  "v_fma_f32 v147, v147, s56, -v233\n"
  "v_exp_f32 v144, v144\n"
  "v_exp_f32 v145, v145\n"
  "v_exp_f32 v146, v146\n"
  "v_exp_f32 v147, v147\n"
  "v_add_f32 v239, v239, v144\n"
  "v_add_f32 v239, v239, v145\n"
  "v_add_f32 v239, v239, v146\n"
  "v_add_f32 v239, v239, v147\n"
  "v_fma_f32 v184, v184, s56, -v233\n"
  "v_fma_f32 v185, v185, s56, -v233\n"
  "v_fma_f32 v186, v186, s56, -v233\n"
  "v_fma_f32 v187, v187, s56, -v233\n"
  "v_exp_f32 v184, v184\n"
  "v_exp_f32 v185, v185\n"
  "v_exp_f32 v186, v186\n"
  "v_exp_f32 v187, v187\n"
  "v_add_f32 v239, v239, v184\n"
  "v_add_f32 v239, v239, v185\n"
  "v_add_f32 v239, v239, v186\n"
  "v_add_f32 v239, v239, v187\n"
  "v_fma_f32 v180, v180, s56, -v232\n"
  "v_fma_f32 v181, v181, s56, -v232\n"
  "v_fma_f32 v182, v182, s56, -v232\n"
  "v_fma_f32 v183, v183, s56, -v232\n"
  "v_exp_f32 v180, v180\n"
  "v_exp_f32 v181, v181\n"
  "v_exp_f32 v182, v182\n"
  "v_exp_f32 v183, v183\n"
  "v_add_f32 v238, v238, v180\n"
  "v_add_f32 v238, v238, v181\n"
  "v_add_f32 v238, v238, v182\n"
  "v_add_f32 v238, v238, v183\n"
  "v_max3_f32 v243, v238, v239, v240\n"
  "v_cmp_nge_f32 vcc, s57, v243\n"
  "s_waitcnt lgkmcnt(0)\n"
  "s_cbranch_vccnz .Lattn_RARE0\n"
  ".p2align 6\n"
  "s_nop 0\n"
  ".Lattn_LOOP:\n"
  "v_mfma_f32_16x16x32_bf16 v[148:151], a[160:163], a[64:67], 0\n"
  "s_add_u32 m0, s61, 98304\n"
  "ds_read_b128 a[220:223], v211 offset:34048\n"
  "v_add_f32 v235, v235, v238\n"
  "v_mfma_f32_16x16x32_bf16 v[148:151], a[164:167], a[68:71], v[148:151]\n"
  "ds_read_b128 a[216:219], v210 offset:34048\n"
  "v_add_f32 v236, v236, v239\n"
  "v_mfma_f32_16x16x32_bf16 v[148:151], a[168:171], a[72:75], v[148:151]\n"
  "buffer_load_dwordx4 v242, s[44:47], s60 offen offset:0 lds\n"
  "v_mfma_f32_16x16x32_bf16 v[148:151], a[172:175], a[76:79], v[148:151]\n"
  "s_waitcnt lgkmcnt(2)\n"
  "ds_read_b128 a[212:215], v209 offset:34048\n"
  "v_add_f32 v237, v237, v240\n"
  "v_mfma_f32_16x16x32_bf16 v[148:151], a[176:179], a[80:83], v[148:151]\n"
  "ds_read_b128 a[208:211], v208 offset:34048\n"
  "v_cvt_pk_bf16_f32 v144, v144, v145\n"
  "v_mfma_f32_16x16x32_bf16 v[148:151], a[180:183], a[84:87], v[148:151]\n"
  "ds_read_b128 a[204:207], v211 offset:33792\n"
  "v_cvt_pk_bf16_f32 v145, v146, v147\n"
  "v_mfma_f32_16x16x32_bf16 v[148:151], a[184:187], a[88:91], v[148:151]\n"
  "buffer_load_dwordx4 v242, s[44:47], s60 offen offset:1024 lds\n"
  "v_mfma_f32_16x16x32_bf16 v[148:151], a[188:191], a[92:95], v[148:151]\n"
  "ds_read_b128 a[200:203], v210 offset:33792\n"
  "v_cvt_pk_bf16_f32 v146, v184, v185\n"
  "v_mfma_f32_16x16x32_bf16 v[152:155], a[188:191], a[124:127], 0\n"
  "ds_read_b128 a[196:199], v209 offset:33792\n"
  "v_cvt_pk_bf16_f32 v147, v186, v187\n"
  "v_mfma_f32_16x16x32_bf16 v[152:155], a[184:187], a[120:123], v[152:155]\n"
  "ds_read_b128 a[192:195], v208 offset:33792\n"
  "v_cvt_pk_bf16_f32 v142, v180, v181\n"
  "v_mfma_f32_16x16x32_bf16 v[152:155], a[180:183], a[116:119], v[152:155]\n"
  "buffer_load_dwordx4 v242, s[44:47], s60 offen offset:2048 lds\n"
  "v_mfma_f32_16x16x32_bf16 v[152:155], a[176:179], a[112:115], v[152:155]\n"
  "v_cvt_pk_bf16_f32 v143, v182, v183\n"
  "v_mfma_f32_16x16x32_bf16 v[152:155], a[172:175], a[108:111], v[152:155]\n"
  "v_mfma_f32_16x16x32_bf16 v[152:155], a[168:171], a[104:107], v[152:155]\n"
  "v_fma_f32 v148, v148, s56, -v232\n"
  "v_fma_f32 v149, v149, s56, -v232\n"
  "v_mfma_f32_16x16x32_bf16 v[152:155], a[164:167], a[100:103], v[152:155]\n"
  "v_exp_f32 v148, v148\n"
  "v_mfma_f32_16x16x32_bf16 v[152:155], a[160:163], a[96:99], v[152:155]\n"
  "v_exp_f32 v149, v149\n"
  "v_mfma_f32_16x16x32_bf16 v[156:159], a[160:163], a[128:131], 0\n"
  "buffer_load_dwordx4 v242, s[44:47], s60 offen offset:3072 lds\n"
  "v_mfma_f32_16x16x32_bf16 v[156:159], a[164:167], a[132:135], v[156:159]\n"
  "s_add_u32 m0, s61, 102400\n"
  "v_fma_f32 v150, v150, s56, -v232\n"
  "v_fma_f32 v151, v151, s56, -v232\n"
  "v_mfma_f32_16x16x32_bf16 v[156:159], a[168:171], a[136:139], v[156:159]\n"
  "s_add_u32 s60, s60, 4096\n"
  "v_exp_f32 v150, v150\n"
  "v_mfma_f32_16x16x32_bf16 v[156:159], a[172:175], a[140:143], v[156:159]\n"
  "v_exp_f32 v151, v151\n"
  "v_mfma_f32_16x16x32_bf16 v[156:159], a[176:179], a[144:147], v[156:159]\n"
  "v_add_f32 v238, v148, v149\n"
  "v_add_f32 v238, v238, v150\n"
  "v_mfma_f32_16x16x32_bf16 v[156:159], a[180:183], a[148:151], v[156:159]\n"
  "v_fma_f32 v152, v152, s56, -v233\n"
  "v_fma_f32 v153, v153, s56, -v233\n"
  "v_mfma_f32_16x16x32_bf16 v[156:159], a[184:187], a[152:155], v[156:159]\n"
  "ds_read_b128 a[160:163], v212 offset:32768\n"
  "ds_read_b128 a[164:167], v213 offset:32768\n"
  "v_mfma_f32_16x16x32_bf16 v[156:159], a[188:191], a[156:159], v[156:159]\n"
  "s_waitcnt lgkmcnt(2)\n"
  "v_exp_f32 v152, v152\n"
  "v_mfma_f32_16x16x32_bf16 v[176:179], a[220:223], a[156:159], 0\n"
  "buffer_load_dwordx4 v242, s[44:47], s60 offen offset:0 lds\n"
  "v_mfma_f32_16x16x32_bf16 v[176:179], a[216:219], a[152:155], v[176:179]\n"
  "v_exp_f32 v153, v153\n"
  "v_mfma_f32_16x16x32_bf16 v[176:179], a[212:215], a[148:151], v[176:179]\n"
  "ds_read_b128 a[168:171], v214 offset:32768\n"
  "v_fma_f32 v154, v154, s56, -v233\n"
  "v_mfma_f32_16x16x32_bf16 v[176:179], a[208:211], a[144:147], v[176:179]\n"
  "v_exp_f32 v154, v154\n"
  "v_mfma_f32_16x16x32_bf16 v[176:179], a[204:207], a[140:143], v[176:179]\n"
  "ds_read_b128 a[172:175], v215 offset:32768\n"
  "v_fma_f32 v155, v155, s56, -v233\n"
  "v_mfma_f32_16x16x32_bf16 v[176:179], a[200:203], a[136:139], v[176:179]\n"
  "v_exp_f32 v155, v155\n"
  "v_mfma_f32_16x16x32_bf16 v[176:179], a[196:199], a[132:135], v[176:179]\n"
  "ds_read_b128 a[176:179], v212 offset:33024\n"
  "v_fma_f32 v156, v156, s56, -v234\n"
  "v_mfma_f32_16x16x32_bf16 v[176:179], a[192:195], a[128:131], v[176:179]\n"
  "v_exp_f32 v156, v156\n"
  "v_mfma_f32_16x16x32_bf16 v[172:175], a[192:195], a[96:99], 0\n"
  "buffer_load_dwordx4 v242, s[44:47], s60 offen offset:1024 lds\n"
  "v_mfma_f32_16x16x32_bf16 v[172:175], a[196:199], a[100:103], v[172:175]\n"
  "ds_read_b128 a[180:183], v213 offset:33024\n"
  "v_fma_f32 v157, v157, s56, -v234\n"
  "v_mfma_f32_16x16x32_bf16 v[172:175], a[200:203], a[104:107], v[172:175]\n"
  "ds_read_b128 a[184:187], v214 offset:33024\n"
  "v_fma_f32 v158, v158, s56, -v234\n"
  "v_mfma_f32_16x16x32_bf16 v[172:175], a[204:207], a[108:111], v[172:175]\n"
  "v_exp_f32 v157, v157\n"
  "v_mfma_f32_16x16x32_bf16 v[172:175], a[208:211], a[112:115], v[172:175]\n"
  "ds_read_b128 a[188:191], v215 offset:33024\n"
  "v_fma_f32 v159, v159, s56, -v234\n"
  "v_mfma_f32_16x16x32_bf16 v[172:175], a[212:215], a[116:119], v[172:175]\n"
  "v_exp_f32 v158, v158\n"
  "v_mfma_f32_16x16x32_bf16 v[172:175], a[216:219], a[120:123], v[172:175]\n"
  "v_exp_f32 v159, v159\n"
  "v_mfma_f32_16x16x32_bf16 v[172:175], a[220:223], a[124:127], v[172:175]\n"
  "v_fma_f32 v176, v176, s56, -v234\n"
  "v_fma_f32 v177, v177, s56, -v234\n"
  "v_mfma_f32_16x16x32_bf16 v[168:171], a[220:223], a[92:95], 0\n"
  "buffer_load_dwordx4 v242, s[44:47], s60 offen offset:2048 lds\n"
  "v_mfma_f32_16x16x32_bf16 v[168:171], a[216:219], a[88:91], v[168:171]\n"
  "ds_read_b128 a[220:223], v215 offset:34048\n"
  "ds_read_b128 a[216:219], v214 offset:34048\n"
  "v_mfma_f32_16x16x32_bf16 v[168:171], a[212:215], a[84:87], v[168:171]\n"
  "v_exp_f32 v176, v176\n"
  "v_mfma_f32_16x16x32_bf16 v[168:171], a[208:211], a[80:83], v[168:171]\n"
  "v_exp_f32 v177, v177\n"
  "v_mfma_f32_16x16x32_bf16 v[168:171], a[204:207], a[76:79], v[168:171]\n"
  "ds_read_b128 a[212:215], v213 offset:34048\n"
  "v_fma_f32 v178, v178, s56, -v234\n"
  "v_mfma_f32_16x16x32_bf16 v[168:171], a[200:203], a[72:75], v[168:171]\n"
  "ds_read_b128 a[208:211], v212 offset:34048\n"
  "v_fma_f32 v179, v179, s56, -v234\n"
  "v_mfma_f32_16x16x32_bf16 v[168:171], a[196:199], a[68:71], v[168:171]\n"
  "v_exp_f32 v178, v178\n"
  "v_mfma_f32_16x16x32_bf16 v[168:171], a[192:195], a[64:67], v[168:171]\n"
  "s_waitcnt lgkmcnt(4)\n"
  "ds_read_b128 a[204:207], v215 offset:33792\n"
  "v_fma_f32 v172, v172, s56, -v233\n"
  "v_mfma_f32_16x16x32_bf16 v[160:163], a[160:163], a[64:67], 0\n"
  "buffer_load_dwordx4 v242, s[44:47], s60 offen offset:3072 lds\n"
  "v_mfma_f32_16x16x32_bf16 v[160:163], a[164:167], a[68:71], v[160:163]\n"
  "s_add_u32 s60, s60, 28672\n"
  "ds_read_b128 a[200:203], v214 offset:33792\n"
  "ds_read_b128 a[196:199], v213 offset:33792\n"
  "v_mfma_f32_16x16x32_bf16 v[160:163], a[168:171], a[72:75], v[160:163]\n"
  "s_and_b32 s60, s60, 0x7ffff\n"
  "v_exp_f32 v179, v179\n"
  "v_mfma_f32_16x16x32_bf16 v[160:163], a[172:175], a[76:79], v[160:163]\n"
  "s_or_b32 s60, s60, s78\n"
  "ds_read_b128 a[192:195], v212 offset:33792\n"
  "v_fma_f32 v173, v173, s56, -v233\n"
  "v_mfma_f32_16x16x32_bf16 v[160:163], a[176:179], a[80:83], v[160:163]\n"
  "s_add_u32 m0, s61, 32768\n"
  "v_exp_f32 v172, v172\n"
  "v_mfma_f32_16x16x32_bf16 v[160:163], a[180:183], a[84:87], v[160:163]\n"
  "v_exp_f32 v173, v173\n"
  "v_mfma_f32_16x16x32_bf16 v[160:163], a[184:187], a[88:91], v[160:163]\n"
  "v_fma_f32 v174, v174, s56, -v233\n"
  "v_fma_f32 v175, v175, s56, -v233\n"
  "v_mfma_f32_16x16x32_bf16 v[160:163], a[188:191], a[92:95], v[160:163]\n"
  "v_exp_f32 v174, v174\n"
  "v_mfma_f32_16x16x32_bf16 v[164:167], a[188:191], a[124:127], 0\n"
  "v_exp_f32 v175, v175\n"
  "v_mfma_f32_16x16x32_bf16 v[164:167], a[184:187], a[120:123], v[164:167]\n"
  "v_fma_f32 v168, v168, s56, -v232\n"
  "v_fma_f32 v169, v169, s56, -v232\n"
  "v_mfma_f32_16x16x32_bf16 v[164:167], a[180:183], a[116:119], v[164:167]\n"
  "v_exp_f32 v168, v168\n"
  "v_mfma_f32_16x16x32_bf16 v[164:167], a[176:179], a[112:115], v[164:167]\n"
  "s_waitcnt vmcnt(17) lgkmcnt(0)\n"
  "v_exp_f32 v169, v169\n"
  "v_mfma_f32_16x16x32_bf16 v[164:167], a[172:175], a[108:111], v[164:167]\n"
  "s_barrier\n"
  "ds_read_b64_tr_b16 a[224:225], v216 offset:0\n"
  "v_fma_f32 v170, v170, s56, -v232\n"
  "v_mfma_f32_16x16x32_bf16 v[164:167], a[168:171], a[104:107], v[164:167]\n"
  "ds_read_b64_tr_b16 a[226:227], v216 offset:1024\n"
  "v_fma_f32 v171, v171, s56, -v232\n"
  "v_mfma_f32_16x16x32_bf16 v[164:167], a[164:167], a[100:103], v[164:167]\n"
  "ds_read_b64_tr_b16 a[228:229], v224 offset:0\n"
  "v_fma_f32 v160, v160, s56, -v232\n"
  "v_mfma_f32_16x16x32_bf16 v[164:167], a[160:163], a[96:99], v[164:167]\n"
  "buffer_load_dwordx4 v242, s[40:43], s59 offen offset:0 nt lds\n"
  "v_mfma_f32_16x16x32_bf16 v[184:187], a[192:195], a[96:99], 0\n"
  "ds_read_b64_tr_b16 a[230:231], v224 offset:1024\n"
  "v_add_f32 v238, v238, v151\n"
  "v_mfma_f32_16x16x32_bf16 v[184:187], a[196:199], a[100:103], v[184:187]\n"
  "ds_read_b64_tr_b16 a[232:233], v217 offset:0\n"
  "v_add_f32 v239, v152, v153\n"
  "v_mfma_f32_16x16x32_bf16 v[184:187], a[200:203], a[104:107], v[184:187]\n"
  "ds_read_b64_tr_b16 a[234:235], v217 offset:1024\n"
  "v_add_f32 v239, v239, v154\n"
  "v_mfma_f32_16x16x32_bf16 v[184:187], a[204:207], a[108:111], v[184:187]\n"
  "ds_read_b64_tr_b16 a[236:237], v225 offset:0\n"
  "v_add_f32 v239, v239, v155\n"
  "v_mfma_f32_16x16x32_bf16 v[184:187], a[208:211], a[112:115], v[184:187]\n"
  "ds_read_b64_tr_b16 a[238:239], v225 offset:1024\n"
  "v_add_f32 v240, v156, v157\n"
  "v_mfma_f32_16x16x32_bf16 v[184:187], a[212:215], a[116:119], v[184:187]\n"
  "ds_read_b64_tr_b16 a[240:241], v218 offset:0\n"
  "v_add_f32 v240, v240, v158\n"
  "v_mfma_f32_16x16x32_bf16 v[184:187], a[216:219], a[120:123], v[184:187]\n"
  "ds_read_b64_tr_b16 a[242:243], v218 offset:1024\n"
  "v_add_f32 v240, v240, v159\n"
  "v_mfma_f32_16x16x32_bf16 v[184:187], a[220:223], a[124:127], v[184:187]\n"
  "ds_read_b64_tr_b16 a[244:245], v226 offset:0\n"
  "v_add_f32 v240, v240, v176\n"
  "v_mfma_f32_16x16x32_bf16 v[180:183], a[220:223], a[92:95], 0\n"
  "buffer_load_dwordx4 v242, s[40:43], s59 offen offset:1024 nt lds\n"
  "v_mfma_f32_16x16x32_bf16 v[180:183], a[216:219], a[88:91], v[180:183]\n"
  "ds_read_b64_tr_b16 a[246:247], v226 offset:1024\n"
  "v_add_f32 v240, v240, v177\n"
  "v_mfma_f32_16x16x32_bf16 v[180:183], a[212:215], a[84:87], v[180:183]\n"
  "ds_read_b64_tr_b16 a[248:249], v219 offset:0\n"
  "v_add_f32 v240, v240, v178\n"
  "v_mfma_f32_16x16x32_bf16 v[180:183], a[208:211], a[80:83], v[180:183]\n"
  "ds_read_b64_tr_b16 a[250:251], v219 offset:1024\n"
  "v_add_f32 v240, v240, v179\n"
  "v_mfma_f32_16x16x32_bf16 v[180:183], a[204:207], a[76:79], v[180:183]\n"
  "ds_read_b64_tr_b16 a[252:253], v227 offset:0\n"
  "v_add_f32 v239, v239, v172\n"
  "v_mfma_f32_16x16x32_bf16 v[180:183], a[200:203], a[72:75], v[180:183]\n"
  "ds_read_b64_tr_b16 a[254:255], v227 offset:1024\n"
  "v_add_f32 v239, v239, v173\n"
  "v_mfma_f32_16x16x32_bf16 v[180:183], a[196:199], a[68:71], v[180:183]\n"
  "ds_read_b64_tr_b16 a[176:177], v220 offset:0\n"
  "v_add_f32 v239, v239, v174\n"
  "v_mfma_f32_16x16x32_bf16 v[180:183], a[192:195], a[64:67], v[180:183]\n"
  "s_waitcnt lgkmcnt(7)\n"
  "ds_read_b64_tr_b16 a[178:179], v220 offset:1024\n"
  "v_add_f32 v239, v239, v175\n"
  "v_mfma_f32_16x16x32_bf16 v[0:3], a[224:227], v[128:131], v[0:3]\n"
  "v_exp_f32 v170, v170\n"
  "v_mfma_f32_16x16x32_bf16 v[0:3], a[228:231], v[140:143], v[0:3]\n"
  "buffer_load_dwordx4 v242, s[40:43], s59 offen offset:2048 nt lds\n"
  "v_mfma_f32_16x16x32_bf16 v[64:67], a[228:231], v[144:147], v[64:67]\n"
  "ds_read_b64_tr_b16 a[180:181], v228 offset:0\n"
  "v_fma_f32 v161, v161, s56, -v232\n"
  "v_mfma_f32_16x16x32_bf16 v[64:67], a[224:227], v[132:135], v[64:67]\n"
  "ds_read_b64_tr_b16 a[182:183], v228 offset:1024\n"
  "v_add_f32 v238, v238, v168\n"
  "v_mfma_f32_16x16x32_bf16 a[0:3], a[224:227], v[136:139], a[0:3]\n"
  "ds_read_b64_tr_b16 a[184:185], v221 offset:0\n"
  "v_add_f32 v238, v238, v169\n"
  "v_mfma_f32_16x16x32_bf16 a[4:7], a[232:235], v[136:139], a[4:7]\n"
  "ds_read_b64_tr_b16 a[186:187], v221 offset:1024\n"
  "v_add_f32 v238, v238, v170\n"
  "v_mfma_f32_16x16x32_bf16 v[68:71], a[232:235], v[132:135], v[68:71]\n"
  "ds_read_b64_tr_b16 a[188:189], v229 offset:0\n"
  "v_cvt_pk_bf16_f32 v148, v148, v149\n"
  "v_mfma_f32_16x16x32_bf16 v[68:71], a[236:239], v[144:147], v[68:71]\n"
  "ds_read_b64_tr_b16 a[190:191], v229 offset:1024\n"
  "v_cvt_pk_bf16_f32 v149, v150, v151\n"
  "v_mfma_f32_16x16x32_bf16 v[4:7], a[236:239], v[140:143], v[4:7]\n"
  "ds_read_b64_tr_b16 a[224:225], v222 offset:0\n"
  "v_cvt_pk_bf16_f32 v152, v152, v153\n"
  "v_mfma_f32_16x16x32_bf16 v[4:7], a[232:235], v[128:131], v[4:7]\n"
  "ds_read_b64_tr_b16 a[226:227], v222 offset:1024\n"
  "v_cvt_pk_bf16_f32 v153, v154, v155\n"
  "v_mfma_f32_16x16x32_bf16 v[8:11], a[240:243], v[128:131], v[8:11]\n"
  "s_waitcnt lgkmcnt(8)\n"
  "buffer_load_dwordx4 v242, s[40:43], s59 offen offset:3072 nt lds\n"
  "v_mfma_f32_16x16x32_bf16 v[8:11], a[244:247], v[140:143], v[8:11]\n"
  "s_add_u32 m0, s61, 36864\n"
  "ds_read_b64_tr_b16 a[228:229], v230 offset:0\n"
  "v_cvt_pk_bf16_f32 v156, v156, v157\n"
  "v_mfma_f32_16x16x32_bf16 v[72:75], a[244:247], v[144:147], v[72:75]\n"
  "s_add_u32 s59, s59, 4096\n"
  "ds_read_b64_tr_b16 a[230:231], v230 offset:1024\n"
  "v_cvt_pk_bf16_f32 v157, v158, v159\n"
  "v_mfma_f32_16x16x32_bf16 v[72:75], a[240:243], v[132:135], v[72:75]\n"
  "ds_read_b64_tr_b16 a[232:233], v223 offset:0\n"
  "v_cvt_pk_bf16_f32 v158, v176, v177\n"
  "v_mfma_f32_16x16x32_bf16 a[8:11], a[240:243], v[136:139], a[8:11]\n"
  "ds_read_b64_tr_b16 a[234:235], v223 offset:1024\n"
  "v_cvt_pk_bf16_f32 v159, v178, v179\n"
  "v_mfma_f32_16x16x32_bf16 a[12:15], a[248:251], v[136:139], a[12:15]\n"
  "ds_read_b64_tr_b16 a[236:237], v231 offset:0\n"
  "v_cvt_pk_bf16_f32 v154, v172, v173\n"
  "v_mfma_f32_16x16x32_bf16 v[76:79], a[248:251], v[132:135], v[76:79]\n"
  "ds_read_b64_tr_b16 a[238:239], v231 offset:1024\n"
  "v_cvt_pk_bf16_f32 v155, v174, v175\n"
  "v_mfma_f32_16x16x32_bf16 v[76:79], a[252:255], v[144:147], v[76:79]\n"
  "ds_read_b64_tr_b16 a[240:241], v216 offset:256\n"
  "v_cvt_pk_bf16_f32 v150, v168, v169\n"
  "v_mfma_f32_16x16x32_bf16 v[12:15], a[252:255], v[140:143], v[12:15]\n"
  "ds_read_b64_tr_b16 a[242:243], v216 offset:1280\n"
  "v_fma_f32 v162, v162, s56, -v232\n"
  "v_mfma_f32_16x16x32_bf16 v[12:15], a[248:251], v[128:131], v[12:15]\n"
  "buffer_load_dwordx4 v242, s[40:43], s59 offen offset:0 nt lds\n"
  "v_mfma_f32_16x16x32_bf16 v[16:19], a[176:179], v[128:131], v[16:19]\n"
  "s_waitcnt lgkmcnt(7)\n"
  "ds_read_b64_tr_b16 a[244:245], v224 offset:256\n"
  "v_fma_f32 v163, v163, s56, -v232\n"
  "v_mfma_f32_16x16x32_bf16 v[16:19], a[180:183], v[140:143], v[16:19]\n"
  "s_add_u32 s58, s58, 1\n"
  "ds_read_b64_tr_b16 a[246:247], v224 offset:1280\n"
  "v_fma_f32 v164, v164, s56, -v233\n"
  "v_mfma_f32_16x16x32_bf16 v[80:83], a[180:183], v[144:147], v[80:83]\n"
  "s_bitcmp1_b32 s69, s58\n"
  "ds_read_b64_tr_b16 a[248:249], v217 offset:256\n"
  "v_fma_f32 v165, v165, s56, -v233\n"
  "v_mfma_f32_16x16x32_bf16 v[80:83], a[176:179], v[132:135], v[80:83]\n"
  "s_cselect_b32 s57, s83, s88\n"
  "ds_read_b64_tr_b16 a[250:251], v217 offset:1280\n"
  "v_fma_f32 v166, v166, s56, -v233\n"
  "v_mfma_f32_16x16x32_bf16 a[16:19], a[176:179], v[136:139], a[16:19]\n"
  "ds_read_b64_tr_b16 a[252:253], v225 offset:256\n"
  "v_fma_f32 v167, v167, s56, -v233\n"
  "v_mfma_f32_16x16x32_bf16 a[20:23], a[184:187], v[136:139], a[20:23]\n"
  "ds_read_b64_tr_b16 a[254:255], v225 offset:1280\n"
  "v_fma_f32 v184, v184, s56, -v233\n"
  "v_mfma_f32_16x16x32_bf16 v[84:87], a[184:187], v[132:135], v[84:87]\n"
  "ds_read_b64_tr_b16 a[176:177], v218 offset:256\n"
  "v_fma_f32 v185, v185, s56, -v233\n"
  "v_mfma_f32_16x16x32_bf16 v[84:87], a[188:191], v[144:147], v[84:87]\n"
  "v_exp_f32 v171, v171\n"
  "v_mfma_f32_16x16x32_bf16 v[20:23], a[188:191], v[140:143], v[20:23]\n"
  "buffer_load_dwordx4 v242, s[40:43], s59 offen offset:1024 nt lds\n"
  "v_mfma_f32_16x16x32_bf16 v[20:23], a[184:187], v[128:131], v[20:23]\n"
  "ds_read_b64_tr_b16 a[178:179], v218 offset:1280\n"
  "v_add_f32 v238, v238, v171\n"
  "v_mfma_f32_16x16x32_bf16 v[24:27], a[224:227], v[128:131], v[24:27]\n"
  "s_waitcnt lgkmcnt(6)\n"
  "ds_read_b64_tr_b16 a[180:181], v226 offset:256\n"
  "v_cvt_pk_bf16_f32 v151, v170, v171\n"
  "v_mfma_f32_16x16x32_bf16 v[24:27], a[228:231], v[140:143], v[24:27]\n"
  "ds_read_b64_tr_b16 a[182:183], v226 offset:1280\n"
  "v_fma_f32 v186, v186, s56, -v233\n"
  "v_mfma_f32_16x16x32_bf16 v[88:91], a[228:231], v[144:147], v[88:91]\n"
  "ds_read_b64_tr_b16 a[184:185], v219 offset:256\n"
  "v_fma_f32 v187, v187, s56, -v233\n"
  "v_mfma_f32_16x16x32_bf16 v[88:91], a[224:227], v[132:135], v[88:91]\n"
  "ds_read_b64_tr_b16 a[186:187], v219 offset:1280\n"
  "v_fma_f32 v180, v180, s56, -v232\n"
  "v_mfma_f32_16x16x32_bf16 a[24:27], a[224:227], v[136:139], a[24:27]\n"
  "ds_read_b64_tr_b16 a[188:189], v227 offset:256\n"
  "v_fma_f32 v181, v181, s56, -v232\n"
  "v_mfma_f32_16x16x32_bf16 a[28:31], a[232:235], v[136:139], a[28:31]\n"
  "ds_read_b64_tr_b16 a[190:191], v227 offset:1280\n"
  "v_fma_f32 v182, v182, s56, -v232\n"
  "v_mfma_f32_16x16x32_bf16 v[92:95], a[232:235], v[132:135], v[92:95]\n"
  "ds_read_b64_tr_b16 a[224:225], v220 offset:256\n"
  "v_fma_f32 v183, v183, s56, -v232\n"
  "v_mfma_f32_16x16x32_bf16 v[92:95], a[236:239], v[144:147], v[92:95]\n"
  "buffer_load_dwordx4 v242, s[40:43], s59 offen offset:2048 nt lds\n"
  "v_mfma_f32_16x16x32_bf16 v[28:31], a[236:239], v[140:143], v[28:31]\n"
  "ds_read_b64_tr_b16 a[226:227], v220 offset:1280\n"
  "ds_read_b64_tr_b16 a[228:229], v228 offset:256\n"
  "v_mfma_f32_16x16x32_bf16 v[28:31], a[232:235], v[128:131], v[28:31]\n"
  "ds_read_b64_tr_b16 a[230:231], v228 offset:1280\n"
  "v_mfma_f32_16x16x32_bf16 v[32:35], a[240:243], v[128:131], v[32:35]\n"
  "v_exp_f32 v160, v160\n"
  "v_mfma_f32_16x16x32_bf16 v[32:35], a[244:247], v[140:143], v[32:35]\n"
  "ds_read_b64_tr_b16 a[232:233], v221 offset:256\n"
  "v_add_f32 v238, v238, v160\n"
  "v_mfma_f32_16x16x32_bf16 v[96:99], a[244:247], v[144:147], v[96:99]\n"
  "ds_read_b64_tr_b16 a[234:235], v221 offset:1280\n"
  "ds_read_b64_tr_b16 a[236:237], v229 offset:256\n"
  "v_mfma_f32_16x16x32_bf16 v[96:99], a[240:243], v[132:135], v[96:99]\n"
  "v_exp_f32 v161, v161\n"
  "v_mfma_f32_16x16x32_bf16 a[32:35], a[240:243], v[136:139], a[32:35]\n"
  "s_waitcnt lgkmcnt(7)\n"
  "ds_read_b64_tr_b16 a[238:239], v229 offset:1280\n"
  "v_add_f32 v238, v238, v161\n"
  "v_mfma_f32_16x16x32_bf16 a[36:39], a[248:251], v[136:139], a[36:39]\n"
  "ds_read_b64_tr_b16 a[240:241], v222 offset:256\n"
  "v_cvt_pk_bf16_f32 v160, v160, v161\n"
  "v_mfma_f32_16x16x32_bf16 v[100:103], a[248:251], v[132:135], v[100:103]\n"
  "buffer_load_dwordx4 v242, s[40:43], s59 offen offset:3072 nt lds\n"
  "v_mfma_f32_16x16x32_bf16 v[100:103], a[252:255], v[144:147], v[100:103]\n"
  "s_add_u32 s59, s59, 28672\n"
  "ds_read_b64_tr_b16 a[242:243], v222 offset:1280\n"
  "ds_read_b64_tr_b16 a[244:245], v230 offset:256\n"
  "v_mfma_f32_16x16x32_bf16 v[36:39], a[252:255], v[140:143], v[36:39]\n"
  "s_and_b32 s59, s59, 0x7ffff\n"
  "v_exp_f32 v162, v162\n"
  "v_mfma_f32_16x16x32_bf16 v[36:39], a[248:251], v[128:131], v[36:39]\n"
  "s_or_b32 s59, s59, s78\n"
  "ds_read_b64_tr_b16 a[246:247], v230 offset:1280\n"
  "v_add_f32 v238, v238, v162\n"
  "v_mfma_f32_16x16x32_bf16 v[40:43], a[176:179], v[128:131], v[40:43]\n"
  "ds_read_b64_tr_b16 a[248:249], v223 offset:256\n"
  "ds_read_b64_tr_b16 a[250:251], v223 offset:1280\n"
  "v_mfma_f32_16x16x32_bf16 v[40:43], a[180:183], v[140:143], v[40:43]\n"
  "v_exp_f32 v163, v163\n"
  "v_mfma_f32_16x16x32_bf16 v[104:107], a[180:183], v[144:147], v[104:107]\n"
  "ds_read_b64_tr_b16 a[252:253], v231 offset:256\n"
  "v_add_f32 v238, v238, v163\n"
  "v_mfma_f32_16x16x32_bf16 v[104:107], a[176:179], v[132:135], v[104:107]\n"
  "buffer_load_dword v254, v253, s[96:99], s68 offen\n"
  "v_mfma_f32_16x16x32_bf16 a[40:43], a[176:179], v[136:139], a[40:43]\n"
  "s_add_u32 s68, s68, 8192\n"
  "ds_read_b64_tr_b16 a[254:255], v231 offset:1280\n"
  "v_cvt_pk_bf16_f32 v161, v162, v163\n"
  "v_mfma_f32_16x16x32_bf16 a[44:47], a[184:187], v[136:139], a[44:47]\n"
  "s_min_u32 s68, s68, 73728\n"
  "v_exp_f32 v164, v164\n"
  "v_mfma_f32_16x16x32_bf16 v[108:111], a[184:187], v[132:135], v[108:111]\n"
  "s_add_u32 s96, s96, s100\n"
  "v_exp_f32 v165, v165\n"
  "v_mfma_f32_16x16x32_bf16 v[108:111], a[188:191], v[144:147], v[108:111]\n"
  "s_addc_u32 s97, s97, s101\n"
  "v_exp_f32 v166, v166\n"
  "v_mfma_f32_16x16x32_bf16 v[44:47], a[188:191], v[140:143], v[44:47]\n"
  "v_exp_f32 v167, v167\n"
  "v_mfma_f32_16x16x32_bf16 v[44:47], a[184:187], v[128:131], v[44:47]\n"
  "s_waitcnt lgkmcnt(2)\n"
  "v_exp_f32 v184, v184\n"
  "v_mfma_f32_16x16x32_bf16 v[48:51], a[224:227], v[128:131], v[48:51]\n"
  "v_exp_f32 v185, v185\n"
  "v_mfma_f32_16x16x32_bf16 v[48:51], a[228:231], v[140:143], v[48:51]\n"
  "v_exp_f32 v186, v186\n"
  "v_mfma_f32_16x16x32_bf16 v[112:115], a[228:231], v[144:147], v[112:115]\n"
  "v_exp_f32 v187, v187\n"
  "v_mfma_f32_16x16x32_bf16 v[112:115], a[224:227], v[132:135], v[112:115]\n"
  "v_exp_f32 v180, v180\n"
  "v_mfma_f32_16x16x32_bf16 a[48:51], a[224:227], v[136:139], a[48:51]\n"
  "v_exp_f32 v181, v181\n"
  "v_mfma_f32_16x16x32_bf16 a[52:55], a[232:235], v[136:139], a[52:55]\n"
  "v_exp_f32 v182, v182\n"
  "v_mfma_f32_16x16x32_bf16 v[116:119], a[232:235], v[132:135], v[116:119]\n"
  "s_waitcnt vmcnt(18) lgkmcnt(0)\n"
  "v_exp_f32 v183, v183\n"
  "v_mfma_f32_16x16x32_bf16 v[116:119], a[236:239], v[144:147], v[116:119]\n"
  "s_barrier\n"
  "ds_read_b128 a[160:163], v208 offset:0\n"
  "v_add_f32 v239, v239, v164\n"
  "v_mfma_f32_16x16x32_bf16 v[52:55], a[236:239], v[140:143], v[52:55]\n"
  "ds_read_b128 a[164:167], v209 offset:0\n"
  "v_add_f32 v239, v239, v165\n"
  "v_mfma_f32_16x16x32_bf16 v[52:55], a[232:235], v[128:131], v[52:55]\n"
  "ds_read_b128 a[168:171], v210 offset:0\n"
  "v_add_f32 v239, v239, v166\n"
  "v_mfma_f32_16x16x32_bf16 v[56:59], a[240:243], v[128:131], v[56:59]\n"
  "ds_read_b128 a[172:175], v211 offset:0\n"
  "v_add_f32 v239, v239, v167\n"
  "v_mfma_f32_16x16x32_bf16 v[56:59], a[244:247], v[140:143], v[56:59]\n"
  "ds_read_b128 a[176:179], v208 offset:256\n"
  "v_add_f32 v239, v239, v184\n"
  "v_mfma_f32_16x16x32_bf16 v[120:123], a[244:247], v[144:147], v[120:123]\n"
  "ds_read_b128 a[180:183], v209 offset:256\n"
  "v_add_f32 v239, v239, v185\n"
  "v_mfma_f32_16x16x32_bf16 v[120:123], a[240:243], v[132:135], v[120:123]\n"
  "ds_read_b128 a[184:187], v210 offset:256\n"
  "v_add_f32 v239, v239, v186\n"
  "v_mfma_f32_16x16x32_bf16 a[56:59], a[240:243], v[136:139], a[56:59]\n"
  "ds_read_b128 a[188:191], v211 offset:256\n"
  "v_add_f32 v239, v239, v187\n"
  "v_mfma_f32_16x16x32_bf16 a[60:63], a[248:251], v[136:139], a[60:63]\n"
  "v_add_f32 v238, v238, v180\n"
  "v_add_f32 v238, v238, v181\n"
  "v_mfma_f32_16x16x32_bf16 v[124:127], a[248:251], v[132:135], v[124:127]\n"
  "v_add_f32 v238, v238, v182\n"
  "v_add_f32 v238, v238, v183\n"
  "v_mfma_f32_16x16x32_bf16 v[124:127], a[252:255], v[144:147], v[124:127]\n"
  "v_max3_f32 v243, v238, v239, v240\n"
  "v_cmp_nge_f32 vcc, s57, v243\n"
  "v_mfma_f32_16x16x32_bf16 v[60:63], a[252:255], v[140:143], v[60:63]\n"
  "v_mfma_f32_16x16x32_bf16 v[60:63], a[248:251], v[128:131], v[60:63]\n"
  "s_waitcnt lgkmcnt(4)\n"
  "s_cbranch_vccnz .Lattn_RARE1\n"
  ".Lattn_RET1:\n"
  "v_mfma_f32_16x16x32_bf16 v[128:131], a[160:163], a[64:67], 0\n"
  "s_add_u32 m0, s61, 65536\n"
  "ds_read_b128 a[220:223], v211 offset:1280\n"
  "v_add_f32 v235, v235, v238\n"
  "v_mfma_f32_16x16x32_bf16 v[128:131], a[164:167], a[68:71], v[128:131]\n"
  "ds_read_b128 a[216:219], v210 offset:1280\n"
  "v_add_f32 v236, v236, v239\n"
  "v_mfma_f32_16x16x32_bf16 v[128:131], a[168:171], a[72:75], v[128:131]\n"
  "buffer_load_dwordx4 v242, s[44:47], s60 offen offset:0 lds\n"
  "v_mfma_f32_16x16x32_bf16 v[128:131], a[172:175], a[76:79], v[128:131]\n"
  "s_waitcnt lgkmcnt(2)\n"
  "ds_read_b128 a[212:215], v209 offset:1280\n"
  "v_add_f32 v237, v237, v240\n"
  "v_mfma_f32_16x16x32_bf16 v[128:131], a[176:179], a[80:83], v[128:131]\n"
  "s_cmp_eq_u32 s58, 14\n"
  "ds_read_b128 a[208:211], v208 offset:1280\n"
  "v_cvt_pk_bf16_f32 v164, v164, v165\n"
  "v_mfma_f32_16x16x32_bf16 v[128:131], a[180:183], a[84:87], v[128:131]\n"
  "s_cmov_b64 s[40:41], s[48:49]\n"
  "ds_read_b128 a[204:207], v211 offset:1024\n"
  "v_cvt_pk_bf16_f32 v165, v166, v167\n"
  "v_mfma_f32_16x16x32_bf16 v[128:131], a[184:187], a[88:91], v[128:131]\n"
  "s_cmov_b32 s59, s80\n"
  "buffer_load_dwordx4 v242, s[44:47], s60 offen offset:1024 lds\n"
  "v_mfma_f32_16x16x32_bf16 v[128:131], a[188:191], a[92:95], v[128:131]\n"
  "s_cmp_eq_u32 s58, 30\n"
  "ds_read_b128 a[200:203], v210 offset:1024\n"
  "v_cvt_pk_bf16_f32 v166, v184, v185\n"
  "v_mfma_f32_16x16x32_bf16 v[132:135], a[188:191], a[124:127], 0\n"
  "s_cmov_b32 s42, 0\n"
  "ds_read_b128 a[196:199], v209 offset:1024\n"
  "v_cvt_pk_bf16_f32 v167, v186, v187\n"
  "v_mfma_f32_16x16x32_bf16 v[132:135], a[184:187], a[120:123], v[132:135]\n"
  "s_cmp_eq_u32 s58, 10\n"
  "ds_read_b128 a[192:195], v208 offset:1024\n"
  "v_cvt_pk_bf16_f32 v162, v180, v181\n"
  "v_mfma_f32_16x16x32_bf16 v[132:135], a[180:183], a[116:119], v[132:135]\n"
  "s_cmov_b64 s[96:97], s[72:73]\n"
  "buffer_load_dwordx4 v242, s[44:47], s60 offen offset:2048 lds\n"
  "v_mfma_f32_16x16x32_bf16 v[132:135], a[176:179], a[112:115], v[132:135]\n"
  "s_cmov_b32 s98, 0x60000\n"
  "v_cvt_pk_bf16_f32 v163, v182, v183\n"
  "v_mfma_f32_16x16x32_bf16 v[132:135], a[172:175], a[108:111], v[132:135]\n"
  "s_cmov_b32 s100, 16384\n"
  "v_mfma_f32_16x16x32_bf16 v[132:135], a[168:171], a[104:107], v[132:135]\n"
  "s_cmp_eq_u32 s58, 22\n"
  "v_fma_f32 v128, v128, s56, -v232\n"
  "v_fma_f32 v129, v129, s56, -v232\n"
  "v_mfma_f32_16x16x32_bf16 v[132:135], a[164:167], a[100:103], v[132:135]\n"
  "s_cmov_b64 s[96:97], s[74:75]\n"
  "v_exp_f32 v128, v128\n"
  "v_mfma_f32_16x16x32_bf16 v[132:135], a[160:163], a[96:99], v[132:135]\n"
  "s_cmov_b32 s98, 0x60000\n"
  "v_exp_f32 v129, v129\n"
  "v_mfma_f32_16x16x32_bf16 v[136:139], a[160:163], a[128:131], 0\n"
  "s_cmov_b32 s100, 16384\n"
  "buffer_load_dwordx4 v242, s[44:47], s60 offen offset:3072 lds\n"
  "v_mfma_f32_16x16x32_bf16 v[136:139], a[164:167], a[132:135], v[136:139]\n"
  "s_add_u32 m0, s61, 69632\n"
  "v_fma_f32 v130, v130, s56, -v232\n"
  "v_fma_f32 v131, v131, s56, -v232\n"
  "v_mfma_f32_16x16x32_bf16 v[136:139], a[168:171], a[136:139], v[136:139]\n"
  "s_add_u32 s60, s60, 4096\n"
  "v_exp_f32 v130, v130\n"
  "v_mfma_f32_16x16x32_bf16 v[136:139], a[172:175], a[140:143], v[136:139]\n"
  "s_cmp_eq_u32 s58, 28\n"
  "v_exp_f32 v131, v131\n"
  "v_mfma_f32_16x16x32_bf16 v[136:139], a[176:179], a[144:147], v[136:139]\n"
  "s_cmov_b32 s98, 0\n"
  "v_add_f32 v238, v128, v129\n"
  "v_add_f32 v238, v238, v130\n"
  "v_mfma_f32_16x16x32_bf16 v[136:139], a[180:183], a[148:151], v[136:139]\n"
  "s_cmov_b32 s100, 0\n"
  "v_fma_f32 v132, v132, s56, -v233\n"
  "v_fma_f32 v133, v133, s56, -v233\n"
  "v_mfma_f32_16x16x32_bf16 v[136:139], a[184:187], a[152:155], v[136:139]\n"
  "ds_read_b128 a[160:163], v212 offset:0\n"
  "ds_read_b128 a[164:167], v213 offset:0\n"
  "v_mfma_f32_16x16x32_bf16 v[136:139], a[188:191], a[156:159], v[136:139]\n"
  "s_waitcnt lgkmcnt(2)\n"
  "v_exp_f32 v132, v132\n"
  "v_mfma_f32_16x16x32_bf16 v[176:179], a[220:223], a[156:159], 0\n"
  "buffer_load_dwordx4 v242, s[44:47], s60 offen offset:0 lds\n"
  "v_mfma_f32_16x16x32_bf16 v[176:179], a[216:219], a[152:155], v[176:179]\n"
  "v_exp_f32 v133, v133\n"
  "v_mfma_f32_16x16x32_bf16 v[176:179], a[212:215], a[148:151], v[176:179]\n"
  "ds_read_b128 a[168:171], v214 offset:0\n"
  "v_fma_f32 v134, v134, s56, -v233\n"
  "v_mfma_f32_16x16x32_bf16 v[176:179], a[208:211], a[144:147], v[176:179]\n"
  "v_exp_f32 v134, v134\n"
  "v_mfma_f32_16x16x32_bf16 v[176:179], a[204:207], a[140:143], v[176:179]\n"
  "ds_read_b128 a[172:175], v215 offset:0\n"
  "v_fma_f32 v135, v135, s56, -v233\n"
  "v_mfma_f32_16x16x32_bf16 v[176:179], a[200:203], a[136:139], v[176:179]\n"
  "v_exp_f32 v135, v135\n"
  "v_mfma_f32_16x16x32_bf16 v[176:179], a[196:199], a[132:135], v[176:179]\n"
  "ds_read_b128 a[176:179], v212 offset:256\n"
  "v_fma_f32 v136, v136, s56, -v234\n"
  "v_mfma_f32_16x16x32_bf16 v[176:179], a[192:195], a[128:131], v[176:179]\n"
  "v_exp_f32 v136, v136\n"
  "v_mfma_f32_16x16x32_bf16 v[172:175], a[192:195], a[96:99], 0\n"
  "buffer_load_dwordx4 v242, s[44:47], s60 offen offset:1024 lds\n"
  "v_mfma_f32_16x16x32_bf16 v[172:175], a[196:199], a[100:103], v[172:175]\n"
  "ds_read_b128 a[180:183], v213 offset:256\n"
  "v_fma_f32 v137, v137, s56, -v234\n"
  "v_mfma_f32_16x16x32_bf16 v[172:175], a[200:203], a[104:107], v[172:175]\n"
  "ds_read_b128 a[184:187], v214 offset:256\n"
  "v_fma_f32 v138, v138, s56, -v234\n"
  "v_mfma_f32_16x16x32_bf16 v[172:175], a[204:207], a[108:111], v[172:175]\n"
  "v_exp_f32 v137, v137\n"
  "v_mfma_f32_16x16x32_bf16 v[172:175], a[208:211], a[112:115], v[172:175]\n"
  "ds_read_b128 a[188:191], v215 offset:256\n"
  "v_fma_f32 v139, v139, s56, -v234\n"
  "v_mfma_f32_16x16x32_bf16 v[172:175], a[212:215], a[116:119], v[172:175]\n"
  "v_exp_f32 v138, v138\n"
  "v_mfma_f32_16x16x32_bf16 v[172:175], a[216:219], a[120:123], v[172:175]\n"
  "v_exp_f32 v139, v139\n"
  "v_mfma_f32_16x16x32_bf16 v[172:175], a[220:223], a[124:127], v[172:175]\n"
  "v_fma_f32 v176, v176, s56, -v234\n"
  "v_fma_f32 v177, v177, s56, -v234\n"
  "v_mfma_f32_16x16x32_bf16 v[168:171], a[220:223], a[92:95], 0\n"
  "buffer_load_dwordx4 v242, s[44:47], s60 offen offset:2048 lds\n"
  "v_mfma_f32_16x16x32_bf16 v[168:171], a[216:219], a[88:91], v[168:171]\n"
  "ds_read_b128 a[220:223], v215 offset:1280\n"
  "ds_read_b128 a[216:219], v214 offset:1280\n"
  "v_mfma_f32_16x16x32_bf16 v[168:171], a[212:215], a[84:87], v[168:171]\n"
  "v_exp_f32 v176, v176\n"
  "v_mfma_f32_16x16x32_bf16 v[168:171], a[208:211], a[80:83], v[168:171]\n"
  "v_exp_f32 v177, v177\n"
  "v_mfma_f32_16x16x32_bf16 v[168:171], a[204:207], a[76:79], v[168:171]\n"
  "ds_read_b128 a[212:215], v213 offset:1280\n"
  "v_fma_f32 v178, v178, s56, -v234\n"
  "v_mfma_f32_16x16x32_bf16 v[168:171], a[200:203], a[72:75], v[168:171]\n"
  "ds_read_b128 a[208:211], v212 offset:1280\n"
  "v_fma_f32 v179, v179, s56, -v234\n"
  "v_mfma_f32_16x16x32_bf16 v[168:171], a[196:199], a[68:71], v[168:171]\n"
  "v_exp_f32 v178, v178\n"
  "v_mfma_f32_16x16x32_bf16 v[168:171], a[192:195], a[64:67], v[168:171]\n"
  "s_waitcnt lgkmcnt(4)\n"
  "ds_read_b128 a[204:207], v215 offset:1024\n"
  "v_fma_f32 v172, v172, s56, -v233\n"
  "v_mfma_f32_16x16x32_bf16 v[140:143], a[160:163], a[64:67], 0\n"
  "buffer_load_dwordx4 v242, s[44:47], s60 offen offset:3072 lds\n"
  "v_mfma_f32_16x16x32_bf16 v[140:143], a[164:167], a[68:71], v[140:143]\n"
  "s_add_u32 s60, s60, 28672\n"
  "ds_read_b128 a[200:203], v214 offset:1024\n"
  "ds_read_b128 a[196:199], v213 offset:1024\n"
  "v_mfma_f32_16x16x32_bf16 v[140:143], a[168:171], a[72:75], v[140:143]\n"
  "s_and_b32 s60, s60, 0x7ffff\n"
  "v_exp_f32 v179, v179\n"
  "v_mfma_f32_16x16x32_bf16 v[140:143], a[172:175], a[76:79], v[140:143]\n"
  "s_or_b32 s60, s60, s78\n"
  "ds_read_b128 a[192:195], v212 offset:1024\n"
  "v_fma_f32 v173, v173, s56, -v233\n"
  "v_mfma_f32_16x16x32_bf16 v[140:143], a[176:179], a[80:83], v[140:143]\n"
  "s_add_u32 m0, s61, 0\n"
  "v_exp_f32 v172, v172\n"
  "v_mfma_f32_16x16x32_bf16 v[140:143], a[180:183], a[84:87], v[140:143]\n"
  "v_exp_f32 v173, v173\n"
  "v_mfma_f32_16x16x32_bf16 v[140:143], a[184:187], a[88:91], v[140:143]\n"
  "v_fma_f32 v174, v174, s56, -v233\n"
  "v_fma_f32 v175, v175, s56, -v233\n"
  "v_mfma_f32_16x16x32_bf16 v[140:143], a[188:191], a[92:95], v[140:143]\n"
  "v_exp_f32 v174, v174\n"
  "v_mfma_f32_16x16x32_bf16 v[144:147], a[188:191], a[124:127], 0\n"
  "v_exp_f32 v175, v175\n"
  "v_mfma_f32_16x16x32_bf16 v[144:147], a[184:187], a[120:123], v[144:147]\n"
  "v_fma_f32 v168, v168, s56, -v232\n"
  "v_fma_f32 v169, v169, s56, -v232\n"
  "v_mfma_f32_16x16x32_bf16 v[144:147], a[180:183], a[116:119], v[144:147]\n"
  "v_exp_f32 v168, v168\n"
  "v_mfma_f32_16x16x32_bf16 v[144:147], a[176:179], a[112:115], v[144:147]\n"
  "s_waitcnt vmcnt(17) lgkmcnt(0)\n"
  "v_exp_f32 v169, v169\n"
  "v_mfma_f32_16x16x32_bf16 v[144:147], a[172:175], a[108:111], v[144:147]\n"
  "s_barrier\n"
  "ds_read_b64_tr_b16 a[224:225], v216 offset:32768\n"
  "v_fma_f32 v170, v170, s56, -v232\n"
  "v_mfma_f32_16x16x32_bf16 v[144:147], a[168:171], a[104:107], v[144:147]\n"
  "ds_read_b64_tr_b16 a[226:227], v216 offset:33792\n"
  "v_fma_f32 v171, v171, s56, -v232\n"
  "v_mfma_f32_16x16x32_bf16 v[144:147], a[164:167], a[100:103], v[144:147]\n"
  "ds_read_b64_tr_b16 a[228:229], v224 offset:32768\n"
  "v_fma_f32 v140, v140, s56, -v232\n"
  "v_mfma_f32_16x16x32_bf16 v[144:147], a[160:163], a[96:99], v[144:147]\n"
  "buffer_load_dwordx4 v242, s[40:43], s59 offen offset:0 nt lds\n"
  "v_mfma_f32_16x16x32_bf16 v[184:187], a[192:195], a[96:99], 0\n"
  "ds_read_b64_tr_b16 a[230:231], v224 offset:33792\n"
  "v_add_f32 v238, v238, v131\n"
  "v_mfma_f32_16x16x32_bf16 v[184:187], a[196:199], a[100:103], v[184:187]\n"
  "ds_read_b64_tr_b16 a[232:233], v217 offset:32768\n"
  "v_add_f32 v239, v132, v133\n"
  "v_mfma_f32_16x16x32_bf16 v[184:187], a[200:203], a[104:107], v[184:187]\n"
  "ds_read_b64_tr_b16 a[234:235], v217 offset:33792\n"
  "v_add_f32 v239, v239, v134\n"
  "v_mfma_f32_16x16x32_bf16 v[184:187], a[204:207], a[108:111], v[184:187]\n"
  "ds_read_b64_tr_b16 a[236:237], v225 offset:32768\n"
  "v_add_f32 v239, v239, v135\n"
  "v_mfma_f32_16x16x32_bf16 v[184:187], a[208:211], a[112:115], v[184:187]\n"
  "ds_read_b64_tr_b16 a[238:239], v225 offset:33792\n"
  "v_add_f32 v240, v136, v137\n"
  "v_mfma_f32_16x16x32_bf16 v[184:187], a[212:215], a[116:119], v[184:187]\n"
  "ds_read_b64_tr_b16 a[240:241], v218 offset:32768\n"
  "v_add_f32 v240, v240, v138\n"
  "v_mfma_f32_16x16x32_bf16 v[184:187], a[216:219], a[120:123], v[184:187]\n"
  "ds_read_b64_tr_b16 a[242:243], v218 offset:33792\n"
  "v_add_f32 v240, v240, v139\n"
  "v_mfma_f32_16x16x32_bf16 v[184:187], a[220:223], a[124:127], v[184:187]\n"
  "ds_read_b64_tr_b16 a[244:245], v226 offset:32768\n"
  "v_add_f32 v240, v240, v176\n"
  "v_mfma_f32_16x16x32_bf16 v[180:183], a[220:223], a[92:95], 0\n"
  "buffer_load_dwordx4 v242, s[40:43], s59 offen offset:1024 nt lds\n"
  "v_mfma_f32_16x16x32_bf16 v[180:183], a[216:219], a[88:91], v[180:183]\n"
  "ds_read_b64_tr_b16 a[246:247], v226 offset:33792\n"
  "v_add_f32 v240, v240, v177\n"
  "v_mfma_f32_16x16x32_bf16 v[180:183], a[212:215], a[84:87], v[180:183]\n"
  "ds_read_b64_tr_b16 a[248:249], v219 offset:32768\n"
  "v_add_f32 v240, v240, v178\n"
  "v_mfma_f32_16x16x32_bf16 v[180:183], a[208:211], a[80:83], v[180:183]\n"
  "ds_read_b64_tr_b16 a[250:251], v219 offset:33792\n"
  "v_add_f32 v240, v240, v179\n"
  "v_mfma_f32_16x16x32_bf16 v[180:183], a[204:207], a[76:79], v[180:183]\n"
  "ds_read_b64_tr_b16 a[252:253], v227 offset:32768\n"
  "v_add_f32 v239, v239, v172\n"
  "v_mfma_f32_16x16x32_bf16 v[180:183], a[200:203], a[72:75], v[180:183]\n"
  "ds_read_b64_tr_b16 a[254:255], v227 offset:33792\n"
  "v_add_f32 v239, v239, v173\n"
  "v_mfma_f32_16x16x32_bf16 v[180:183], a[196:199], a[68:71], v[180:183]\n"
  "ds_read_b64_tr_b16 a[176:177], v220 offset:32768\n"
  "v_add_f32 v239, v239, v174\n"
  "v_mfma_f32_16x16x32_bf16 v[180:183], a[192:195], a[64:67], v[180:183]\n"
  "s_waitcnt lgkmcnt(7)\n"
  "ds_read_b64_tr_b16 a[178:179], v220 offset:33792\n"
  "v_add_f32 v239, v239, v175\n"
  "v_mfma_f32_16x16x32_bf16 v[0:3], a[224:227], v[148:151], v[0:3]\n"
  "v_exp_f32 v170, v170\n"
  "v_mfma_f32_16x16x32_bf16 v[0:3], a[228:231], v[160:163], v[0:3]\n"
  "buffer_load_dwordx4 v242, s[40:43], s59 offen offset:2048 nt lds\n"
  "v_mfma_f32_16x16x32_bf16 v[64:67], a[228:231], v[164:167], v[64:67]\n"
  "ds_read_b64_tr_b16 a[180:181], v228 offset:32768\n"
  "v_fma_f32 v141, v141, s56, -v232\n"
  "v_mfma_f32_16x16x32_bf16 v[64:67], a[224:227], v[152:155], v[64:67]\n"
  "ds_read_b64_tr_b16 a[182:183], v228 offset:33792\n"
  "v_add_f32 v238, v238, v168\n"
  "v_mfma_f32_16x16x32_bf16 a[0:3], a[224:227], v[156:159], a[0:3]\n"
  "ds_read_b64_tr_b16 a[184:185], v221 offset:32768\n"
  "v_add_f32 v238, v238, v169\n"
  "v_mfma_f32_16x16x32_bf16 a[4:7], a[232:235], v[156:159], a[4:7]\n"
  "ds_read_b64_tr_b16 a[186:187], v221 offset:33792\n"
  "v_add_f32 v238, v238, v170\n"
  "v_mfma_f32_16x16x32_bf16 v[68:71], a[232:235], v[152:155], v[68:71]\n"
  "ds_read_b64_tr_b16 a[188:189], v229 offset:32768\n"
  "v_cvt_pk_bf16_f32 v128, v128, v129\n"
  "v_mfma_f32_16x16x32_bf16 v[68:71], a[236:239], v[164:167], v[68:71]\n"
  "ds_read_b64_tr_b16 a[190:191], v229 offset:33792\n"
  "v_cvt_pk_bf16_f32 v129, v130, v131\n"
  "v_mfma_f32_16x16x32_bf16 v[4:7], a[236:239], v[160:163], v[4:7]\n"
  "ds_read_b64_tr_b16 a[224:225], v222 offset:32768\n"
  "v_cvt_pk_bf16_f32 v132, v132, v133\n"
  "v_mfma_f32_16x16x32_bf16 v[4:7], a[232:235], v[148:151], v[4:7]\n"
  "ds_read_b64_tr_b16 a[226:227], v222 offset:33792\n"
  "v_cvt_pk_bf16_f32 v133, v134, v135\n"
  "v_mfma_f32_16x16x32_bf16 v[8:11], a[240:243], v[148:151], v[8:11]\n"
  "s_waitcnt lgkmcnt(8)\n"
  "buffer_load_dwordx4 v242, s[40:43], s59 offen offset:3072 nt lds\n"
  "v_mfma_f32_16x16x32_bf16 v[8:11], a[244:247], v[160:163], v[8:11]\n"
  "s_add_u32 m0, s61, 4096\n"
  "ds_read_b64_tr_b16 a[228:229], v230 offset:32768\n"
  "v_cvt_pk_bf16_f32 v136, v136, v137\n"
  "v_mfma_f32_16x16x32_bf16 v[72:75], a[244:247], v[164:167], v[72:75]\n"
  "s_add_u32 s59, s59, 4096\n"
  "ds_read_b64_tr_b16 a[230:231], v230 offset:33792\n"
  "v_cvt_pk_bf16_f32 v137, v138, v139\n"
  "v_mfma_f32_16x16x32_bf16 v[72:75], a[240:243], v[152:155], v[72:75]\n"
  "ds_read_b64_tr_b16 a[232:233], v223 offset:32768\n"
  "v_cvt_pk_bf16_f32 v138, v176, v177\n"
  "v_mfma_f32_16x16x32_bf16 a[8:11], a[240:243], v[156:159], a[8:11]\n"
  "ds_read_b64_tr_b16 a[234:235], v223 offset:33792\n"
  "v_cvt_pk_bf16_f32 v139, v178, v179\n"
  "v_mfma_f32_16x16x32_bf16 a[12:15], a[248:251], v[156:159], a[12:15]\n"
  "ds_read_b64_tr_b16 a[236:237], v231 offset:32768\n"
  "v_cvt_pk_bf16_f32 v134, v172, v173\n"
  "v_mfma_f32_16x16x32_bf16 v[76:79], a[248:251], v[152:155], v[76:79]\n"
  "ds_read_b64_tr_b16 a[238:239], v231 offset:33792\n"
  "v_cvt_pk_bf16_f32 v135, v174, v175\n"
  "v_mfma_f32_16x16x32_bf16 v[76:79], a[252:255], v[164:167], v[76:79]\n"
  "ds_read_b64_tr_b16 a[240:241], v216 offset:33024\n"
  "v_cvt_pk_bf16_f32 v130, v168, v169\n"
  "v_mfma_f32_16x16x32_bf16 v[12:15], a[252:255], v[160:163], v[12:15]\n"
  "ds_read_b64_tr_b16 a[242:243], v216 offset:34048\n"
  "v_fma_f32 v142, v142, s56, -v232\n"
  "v_mfma_f32_16x16x32_bf16 v[12:15], a[248:251], v[148:151], v[12:15]\n"
  "buffer_load_dwordx4 v242, s[40:43], s59 offen offset:0 nt lds\n"
  "v_mfma_f32_16x16x32_bf16 v[16:19], a[176:179], v[148:151], v[16:19]\n"
  "s_waitcnt lgkmcnt(7)\n"
  "ds_read_b64_tr_b16 a[244:245], v224 offset:33024\n"
  "v_fma_f32 v143, v143, s56, -v232\n"
  "v_mfma_f32_16x16x32_bf16 v[16:19], a[180:183], v[160:163], v[16:19]\n"
  "s_add_u32 s58, s58, 1\n"
  "ds_read_b64_tr_b16 a[246:247], v224 offset:34048\n"
  "v_fma_f32 v144, v144, s56, -v233\n"
  "v_mfma_f32_16x16x32_bf16 v[80:83], a[180:183], v[164:167], v[80:83]\n"
  "s_mov_b32 s57, s88\n"
  "ds_read_b64_tr_b16 a[248:249], v217 offset:33024\n"
  "v_fma_f32 v145, v145, s56, -v233\n"
  "v_mfma_f32_16x16x32_bf16 v[80:83], a[176:179], v[152:155], v[80:83]\n"
  "ds_read_b64_tr_b16 a[250:251], v217 offset:34048\n"
  "v_fma_f32 v146, v146, s56, -v233\n"
  "v_mfma_f32_16x16x32_bf16 a[16:19], a[176:179], v[156:159], a[16:19]\n"
  "ds_read_b64_tr_b16 a[252:253], v225 offset:33024\n"
  "v_fma_f32 v147, v147, s56, -v233\n"
  "v_mfma_f32_16x16x32_bf16 a[20:23], a[184:187], v[156:159], a[20:23]\n"
  "ds_read_b64_tr_b16 a[254:255], v225 offset:34048\n"
  "v_fma_f32 v184, v184, s56, -v233\n"
  "v_mfma_f32_16x16x32_bf16 v[84:87], a[184:187], v[152:155], v[84:87]\n"
  "ds_read_b64_tr_b16 a[176:177], v218 offset:33024\n"
  "v_fma_f32 v185, v185, s56, -v233\n"
  "v_mfma_f32_16x16x32_bf16 v[84:87], a[188:191], v[164:167], v[84:87]\n"
  "v_exp_f32 v171, v171\n"
  "v_mfma_f32_16x16x32_bf16 v[20:23], a[188:191], v[160:163], v[20:23]\n"
  "buffer_load_dwordx4 v242, s[40:43], s59 offen offset:1024 nt lds\n"
  "v_mfma_f32_16x16x32_bf16 v[20:23], a[184:187], v[148:151], v[20:23]\n"
  "ds_read_b64_tr_b16 a[178:179], v218 offset:34048\n"
  "v_add_f32 v238, v238, v171\n"
  "v_mfma_f32_16x16x32_bf16 v[24:27], a[224:227], v[148:151], v[24:27]\n"
  "s_waitcnt lgkmcnt(6)\n"
  "ds_read_b64_tr_b16 a[180:181], v226 offset:33024\n"
  "v_cvt_pk_bf16_f32 v131, v170, v171\n"
  "v_mfma_f32_16x16x32_bf16 v[24:27], a[228:231], v[160:163], v[24:27]\n"
  "ds_read_b64_tr_b16 a[182:183], v226 offset:34048\n"
  "v_fma_f32 v186, v186, s56, -v233\n"
  "v_mfma_f32_16x16x32_bf16 v[88:91], a[228:231], v[164:167], v[88:91]\n"
  "ds_read_b64_tr_b16 a[184:185], v219 offset:33024\n"
  "v_fma_f32 v187, v187, s56, -v233\n"
  "v_mfma_f32_16x16x32_bf16 v[88:91], a[224:227], v[152:155], v[88:91]\n"
  "ds_read_b64_tr_b16 a[186:187], v219 offset:34048\n"
  "v_fma_f32 v180, v180, s56, -v232\n"
  "v_mfma_f32_16x16x32_bf16 a[24:27], a[224:227], v[156:159], a[24:27]\n"
  "ds_read_b64_tr_b16 a[188:189], v227 offset:33024\n"
  "v_fma_f32 v181, v181, s56, -v232\n"
  "v_mfma_f32_16x16x32_bf16 a[28:31], a[232:235], v[156:159], a[28:31]\n"
  "ds_read_b64_tr_b16 a[190:191], v227 offset:34048\n"
  "v_fma_f32 v182, v182, s56, -v232\n"
  "v_mfma_f32_16x16x32_bf16 v[92:95], a[232:235], v[152:155], v[92:95]\n"
  "s_nop 0\n"
  "ds_read_b64_tr_b16 a[224:225], v220 offset:33024\n"
  "v_fma_f32 v183, v183, s56, -v232\n"
  "v_mfma_f32_16x16x32_bf16 v[92:95], a[236:239], v[164:167], v[92:95]\n"
  "buffer_load_dwordx4 v242, s[40:43], s59 offen offset:2048 nt lds\n"
  "v_mfma_f32_16x16x32_bf16 v[28:31], a[236:239], v[160:163], v[28:31]\n"
  "ds_read_b64_tr_b16 a[226:227], v220 offset:34048\n"
  "ds_read_b64_tr_b16 a[228:229], v228 offset:33024\n"
  "v_mfma_f32_16x16x32_bf16 v[28:31], a[232:235], v[148:151], v[28:31]\n"
  "ds_read_b64_tr_b16 a[230:231], v228 offset:34048\n"
  "v_mfma_f32_16x16x32_bf16 v[32:35], a[240:243], v[148:151], v[32:35]\n"
  "v_exp_f32 v140, v140\n"
  "v_mfma_f32_16x16x32_bf16 v[32:35], a[244:247], v[160:163], v[32:35]\n"
  "ds_read_b64_tr_b16 a[232:233], v221 offset:33024\n"
  "v_add_f32 v238, v238, v140\n"
  "v_mfma_f32_16x16x32_bf16 v[96:99], a[244:247], v[164:167], v[96:99]\n"
  "ds_read_b64_tr_b16 a[234:235], v221 offset:34048\n"
  "ds_read_b64_tr_b16 a[236:237], v229 offset:33024\n"
  "v_mfma_f32_16x16x32_bf16 v[96:99], a[240:243], v[152:155], v[96:99]\n"
  "v_exp_f32 v141, v141\n"
  "v_mfma_f32_16x16x32_bf16 a[32:35], a[240:243], v[156:159], a[32:35]\n"
  "s_waitcnt lgkmcnt(7)\n"
  "ds_read_b64_tr_b16 a[238:239], v229 offset:34048\n"
  "v_add_f32 v238, v238, v141\n"
  "v_mfma_f32_16x16x32_bf16 a[36:39], a[248:251], v[156:159], a[36:39]\n"
  "ds_read_b64_tr_b16 a[240:241], v222 offset:33024\n"
  "v_cvt_pk_bf16_f32 v140, v140, v141\n"
  "v_mfma_f32_16x16x32_bf16 v[100:103], a[248:251], v[152:155], v[100:103]\n"
  "buffer_load_dwordx4 v242, s[40:43], s59 offen offset:3072 nt lds\n"
  "v_mfma_f32_16x16x32_bf16 v[100:103], a[252:255], v[164:167], v[100:103]\n"
  "s_add_u32 s59, s59, 28672\n"
  "ds_read_b64_tr_b16 a[242:243], v222 offset:34048\n"
  "ds_read_b64_tr_b16 a[244:245], v230 offset:33024\n"
  "v_mfma_f32_16x16x32_bf16 v[36:39], a[252:255], v[160:163], v[36:39]\n"
  "s_and_b32 s59, s59, 0x7ffff\n"
  "v_exp_f32 v142, v142\n"
  "v_mfma_f32_16x16x32_bf16 v[36:39], a[248:251], v[148:151], v[36:39]\n"
  "s_or_b32 s59, s59, s78\n"
  "ds_read_b64_tr_b16 a[246:247], v230 offset:34048\n"
  "v_add_f32 v238, v238, v142\n"
  "v_mfma_f32_16x16x32_bf16 v[40:43], a[176:179], v[148:151], v[40:43]\n"
  "ds_read_b64_tr_b16 a[248:249], v223 offset:33024\n"
  "ds_read_b64_tr_b16 a[250:251], v223 offset:34048\n"
  "v_mfma_f32_16x16x32_bf16 v[40:43], a[180:183], v[160:163], v[40:43]\n"
  "v_exp_f32 v143, v143\n"
  "v_mfma_f32_16x16x32_bf16 v[104:107], a[180:183], v[164:167], v[104:107]\n"
  "ds_read_b64_tr_b16 a[252:253], v231 offset:33024\n"
  "v_add_f32 v238, v238, v143\n"
  "v_mfma_f32_16x16x32_bf16 v[104:107], a[176:179], v[152:155], v[104:107]\n"
  "buffer_load_dword v254, v253, s[96:99], s68 offen\n"
  "v_mfma_f32_16x16x32_bf16 a[40:43], a[176:179], v[156:159], a[40:43]\n"
  "s_add_u32 s68, s68, 8192\n"
  "ds_read_b64_tr_b16 a[254:255], v231 offset:34048\n"
  "v_cvt_pk_bf16_f32 v141, v142, v143\n"
  "v_mfma_f32_16x16x32_bf16 a[44:47], a[184:187], v[156:159], a[44:47]\n"
  "s_min_u32 s68, s68, 73728\n"
  "v_exp_f32 v144, v144\n"
  "v_mfma_f32_16x16x32_bf16 v[108:111], a[184:187], v[152:155], v[108:111]\n"
  "s_add_u32 s96, s96, s100\n"
  "v_exp_f32 v145, v145\n"
  "v_mfma_f32_16x16x32_bf16 v[108:111], a[188:191], v[164:167], v[108:111]\n"
  "s_addc_u32 s97, s97, s101\n"
  "v_exp_f32 v146, v146\n"
  "v_mfma_f32_16x16x32_bf16 v[44:47], a[188:191], v[160:163], v[44:47]\n"
  "s_nop 0\n"
  "v_exp_f32 v147, v147\n"
  "v_mfma_f32_16x16x32_bf16 v[44:47], a[184:187], v[148:151], v[44:47]\n"
  "s_waitcnt lgkmcnt(2)\n"
  "v_exp_f32 v184, v184\n"
  "v_mfma_f32_16x16x32_bf16 v[48:51], a[224:227], v[148:151], v[48:51]\n"
  "v_exp_f32 v185, v185\n"
  "v_mfma_f32_16x16x32_bf16 v[48:51], a[228:231], v[160:163], v[48:51]\n"
  "v_exp_f32 v186, v186\n"
  "v_mfma_f32_16x16x32_bf16 v[112:115], a[228:231], v[164:167], v[112:115]\n"
  "v_exp_f32 v187, v187\n"
  "v_mfma_f32_16x16x32_bf16 v[112:115], a[224:227], v[152:155], v[112:115]\n"
  "v_exp_f32 v180, v180\n"
  "v_mfma_f32_16x16x32_bf16 a[48:51], a[224:227], v[156:159], a[48:51]\n"
  "v_exp_f32 v181, v181\n"
  "v_mfma_f32_16x16x32_bf16 a[52:55], a[232:235], v[156:159], a[52:55]\n"
  "v_exp_f32 v182, v182\n"
  "v_mfma_f32_16x16x32_bf16 v[116:119], a[232:235], v[152:155], v[116:119]\n"
  "s_waitcnt vmcnt(18) lgkmcnt(0)\n"
  "v_exp_f32 v183, v183\n"
  "v_mfma_f32_16x16x32_bf16 v[116:119], a[236:239], v[164:167], v[116:119]\n"
  "s_barrier\n"
  "ds_read_b128 a[160:163], v208 offset:32768\n"
  "v_add_f32 v239, v239, v144\n"
  "v_mfma_f32_16x16x32_bf16 v[52:55], a[236:239], v[160:163], v[52:55]\n"
  "ds_read_b128 a[164:167], v209 offset:32768\n"
  "v_add_f32 v239, v239, v145\n"
  "v_mfma_f32_16x16x32_bf16 v[52:55], a[232:235], v[148:151], v[52:55]\n"
  "ds_read_b128 a[168:171], v210 offset:32768\n"
  "v_add_f32 v239, v239, v146\n"
  "v_mfma_f32_16x16x32_bf16 v[56:59], a[240:243], v[148:151], v[56:59]\n"
  "ds_read_b128 a[172:175], v211 offset:32768\n"
  "v_add_f32 v239, v239, v147\n"
  "v_mfma_f32_16x16x32_bf16 v[56:59], a[244:247], v[160:163], v[56:59]\n"
  "ds_read_b128 a[176:179], v208 offset:33024\n"
  "v_add_f32 v239, v239, v184\n"
  "v_mfma_f32_16x16x32_bf16 v[120:123], a[244:247], v[164:167], v[120:123]\n"
  "ds_read_b128 a[180:183], v209 offset:33024\n"
  "v_add_f32 v239, v239, v185\n"
  "v_mfma_f32_16x16x32_bf16 v[120:123], a[240:243], v[152:155], v[120:123]\n"
  "ds_read_b128 a[184:187], v210 offset:33024\n"
  "v_add_f32 v239, v239, v186\n"
  "v_mfma_f32_16x16x32_bf16 a[56:59], a[240:243], v[156:159], a[56:59]\n"
  "ds_read_b128 a[188:191], v211 offset:33024\n"
  "v_add_f32 v239, v239, v187\n"
  "v_mfma_f32_16x16x32_bf16 a[60:63], a[248:251], v[156:159], a[60:63]\n"
  "v_add_f32 v238, v238, v180\n"
  "v_add_f32 v238, v238, v181\n"
  "v_mfma_f32_16x16x32_bf16 v[124:127], a[248:251], v[152:155], v[124:127]\n"
  "v_add_f32 v238, v238, v182\n"
  "v_add_f32 v238, v238, v183\n"
  "v_mfma_f32_16x16x32_bf16 v[124:127], a[252:255], v[164:167], v[124:127]\n"
  "v_max3_f32 v243, v238, v239, v240\n"
  "v_cmp_nge_f32 vcc, s57, v243\n"
  "v_mfma_f32_16x16x32_bf16 v[60:63], a[252:255], v[160:163], v[60:63]\n"
  "v_mfma_f32_16x16x32_bf16 v[60:63], a[248:251], v[148:151], v[60:63]\n"
  "s_waitcnt lgkmcnt(4)\n"
  "s_cbranch_vccz .Lattn_LOOP\n"
  "s_branch .Lattn_RARE0\n"
  ".Lattn_RARE1:\n"
  "v_cmp_nge_f32 vcc, s88, v243\n"
  "s_nop 3\n"
  "s_cbranch_vccnz .Lattn_RARE1B\n"
  ".Lattn_FINAL:\n"
  "s_mov_b32 s40, s90\n"
  "s_mov_b32 s41, s91\n"
  "s_mov_b32 s78, 524288\n"
  "s_or_b32 s59, s77, s78\n"
  "s_mov_b32 s60, s59\n"
  "s_lshr_b32 s64, s58, 4\n"
  "s_and_b32 s64, s64, 2\n"
  "s_add_u32 s64, s64, s70\n"
  "s_waitcnt vmcnt(9) lgkmcnt(0)\n"
  "s_barrier\n"
  "s_cmp_eq_u32 s64, 0\n"
  "s_cbranch_scc1 .Lattn_FINAL0\n"
  "s_cmp_eq_u32 s64, 2\n"
  "s_cbranch_scc1 .Lattn_FINAL2\n"
  "s_cmp_eq_u32 s64, 3\n"
  "s_cbranch_scc1 .Lattn_FINAL3\n"
  ".Lattn_FINAL1:\n"
  "ds_read_b64_tr_b16 a[224:225], v216 offset:32768\n"
  "ds_read_b64_tr_b16 a[226:227], v216 offset:33792\n"
  "ds_read_b64_tr_b16 a[228:229], v224 offset:32768\n"
  "ds_read_b64_tr_b16 a[230:231], v224 offset:33792\n"
  "ds_read_b64_tr_b16 a[232:233], v217 offset:32768\n"
  "ds_read_b64_tr_b16 a[234:235], v217 offset:33792\n"
  "ds_read_b64_tr_b16 a[236:237], v225 offset:32768\n"
  "ds_read_b64_tr_b16 a[238:239], v225 offset:33792\n"
  "ds_read_b64_tr_b16 a[240:241], v218 offset:32768\n"
  "ds_read_b64_tr_b16 a[242:243], v218 offset:33792\n"
  "ds_read_b64_tr_b16 a[244:245], v226 offset:32768\n"
  "ds_read_b64_tr_b16 a[246:247], v226 offset:33792\n"
  "ds_read_b64_tr_b16 a[248:249], v219 offset:32768\n"
  "ds_read_b64_tr_b16 a[250:251], v219 offset:33792\n"
  "ds_read_b64_tr_b16 a[252:253], v227 offset:32768\n"
  "ds_read_b64_tr_b16 a[254:255], v227 offset:33792\n"
  "ds_read_b64_tr_b16 a[176:177], v220 offset:32768\n"
  "ds_read_b64_tr_b16 a[178:179], v220 offset:33792\n"
  "v_cvt_pk_bf16_f32 v164, v164, v165\n"
  "v_cvt_pk_bf16_f32 v165, v166, v167\n"
  "v_cvt_pk_bf16_f32 v166, v184, v185\n"
  "v_cvt_pk_bf16_f32 v167, v186, v187\n"
  "v_cvt_pk_bf16_f32 v162, v180, v181\n"
  "v_cvt_pk_bf16_f32 v163, v182, v183\n"
  "v_add_f32 v235, v235, v238\n"
  "v_add_f32 v236, v236, v239\n"
  "v_add_f32 v237, v237, v240\n"
  "v_mov_b32 v138, v235\n"
  "v_mov_b32 v139, v236\n"
  "v_mov_b32 v140, v237\n"
  "s_waitcnt lgkmcnt(0)\n"
  "v_mfma_f32_16x16x32_bf16 v[0:3], a[224:227], v[148:151], v[0:3]\n"
  "ds_read_b128 a[64:67], v199 offset:0\n"
  "ds_read_b128 a[68:71], v200 offset:0\n"
  "ds_read_b128 a[72:75], v201 offset:0\n"
  "v_permlane32_swap_b32 v235, v138\n"
  "v_permlane32_swap_b32 v236, v139\n"
  "v_permlane32_swap_b32 v237, v140\n"
  "v_add_f32 v235, v235, v138\n"
  "s_add_u32 m0, s93, 0\n"
  "v_mfma_f32_16x16x32_bf16 v[0:3], a[228:231], v[160:163], v[0:3]\n"
  "ds_read_b128 a[76:79], v202 offset:0\n"
  "ds_read_b128 a[80:83], v199 offset:256\n"
  "ds_read_b128 a[84:87], v200 offset:256\n"
  "buffer_load_dwordx4 v190, s[48:51], 0 offen offset:0 nt lds\n"
  "v_add_f32 v236, v236, v139\n"
  "v_add_f32 v237, v237, v140\n"
  "v_mov_b32 v138, v235\n"
  "v_mov_b32 v139, v236\n"
  "s_add_u32 m0, s93, 1024\n"
  "v_mfma_f32_16x16x32_bf16 v[64:67], a[228:231], v[164:167], v[64:67]\n"
  "ds_read_b64_tr_b16 a[180:181], v228 offset:32768\n"
  "ds_read_b128 a[88:91], v201 offset:256\n"
  "ds_read_b128 a[92:95], v202 offset:256\n"
  "v_mov_b32 v140, v237\n"
  "v_permlane16_swap_b32 v235, v138\n"
  "v_permlane16_swap_b32 v236, v139\n"
  "v_permlane16_swap_b32 v237, v140\n"
  "v_mfma_f32_16x16x32_bf16 v[64:67], a[224:227], v[152:155], v[64:67]\n"
  "ds_read_b64_tr_b16 a[182:183], v228 offset:33792\n"
  "buffer_load_dwordx4 v190, s[48:51], s82 offen offset:0 nt lds\n"
  "v_add_f32 v235, v235, v138\n"
  "v_add_f32 v236, v236, v139\n"
  "v_add_f32 v237, v237, v140\n"
  "v_mov_b32 v130, v234\n"
  "s_add_u32 m0, s93, 1920\n"
  "v_mfma_f32_16x16x32_bf16 a[0:3], a[224:227], v[156:159], a[0:3]\n"
  "ds_read_b64_tr_b16 a[184:185], v221 offset:32768\n"
  "v_mov_b32 v131, v237\n"
  "v_xor_b32 v137, 0x2000, v191\n"
  "v_rcp_f32 v134, v235\n"
  "v_rcp_f32 v135, v236\n"
  "v_mfma_f32_16x16x32_bf16 a[4:7], a[232:235], v[156:159], a[4:7]\n"
  "ds_read_b64_tr_b16 a[186:187], v221 offset:33792\n"
  "ds_write_b64 v137, v[130:131] offset:7168\n"
  "v_mfma_f32_16x16x32_bf16 v[68:71], a[232:235], v[152:155], v[68:71]\n"
  "ds_read_b64_tr_b16 a[188:189], v229 offset:32768\n"
  "buffer_load_dwordx4 v190, s[48:51], 0 offen offset:128 nt lds\n"
  "s_add_u32 m0, s93, 2944\n"
  "v_mfma_f32_16x16x32_bf16 v[68:71], a[236:239], v[164:167], v[68:71]\n"
  "ds_read_b64_tr_b16 a[190:191], v229 offset:33792\n"
  "v_mul_f32 v0, v0, v134\n"
  "v_mul_f32 v1, v1, v134\n"
  "v_mul_f32 v2, v2, v134\n"
  "v_mul_f32 v3, v3, v134\n"
  "v_mfma_f32_16x16x32_bf16 v[4:7], a[236:239], v[160:163], v[4:7]\n"
  "ds_read_b64_tr_b16 a[224:225], v222 offset:32768\n"
  "ds_write_b128 v191, a[0:3] offset:0\n"
  "v_cvt_pk_bf16_f32 v0, v0, v1\n"
  "v_cvt_pk_bf16_f32 v1, v2, v3\n"
  "v_mul_f32 v64, v64, v135\n"
  "v_mul_f32 v65, v65, v135\n"
  "v_mfma_f32_16x16x32_bf16 v[4:7], a[232:235], v[148:151], v[4:7]\n"
  "ds_read_b64_tr_b16 a[226:227], v222 offset:33792\n"
  "buffer_load_dwordx4 v190, s[48:51], s82 offen offset:128 nt lds\n"
  "v_mul_f32 v66, v66, v135\n"
  "v_mul_f32 v67, v67, v135\n"
  "v_cvt_pk_bf16_f32 v64, v64, v65\n"
  "v_cvt_pk_bf16_f32 v65, v66, v67\n"
  "v_mfma_f32_16x16x32_bf16 v[8:11], a[240:243], v[148:151], v[8:11]\n"
  "s_waitcnt lgkmcnt(0)\n"
  "ds_write2st64_b64 v192, v[0:1], v[64:65] offset0:0 offset1:4\n"
  "v_mfma_f32_16x16x32_bf16 v[8:11], a[244:247], v[160:163], v[8:11]\n"
  "s_barrier\n"
  "ds_read_b64_tr_b16 a[228:229], v230 offset:32768\n"
  "s_add_u32 m0, s61, 0\n"
  "v_mfma_f32_16x16x32_bf16 v[72:75], a[244:247], v[164:167], v[72:75]\n"
  "ds_read_b64_tr_b16 a[230:231], v230 offset:33792\n"
  "buffer_load_dwordx4 v242, s[40:43], s59 offen offset:0 nt lds\n"
  "v_mul_f32 v4, v4, v134\n"
  "v_mul_f32 v5, v5, v134\n"
  "v_mul_f32 v6, v6, v134\n"
  "v_mul_f32 v7, v7, v134\n"
  "v_mfma_f32_16x16x32_bf16 v[72:75], a[240:243], v[152:155], v[72:75]\n"
  "ds_read_b64_tr_b16 a[232:233], v223 offset:32768\n"
  "ds_write_b128 v191, a[4:7] offset:1024\n"
  "v_cvt_pk_bf16_f32 v4, v4, v5\n"
  "v_cvt_pk_bf16_f32 v5, v6, v7\n"
  "v_mul_f32 v68, v68, v135\n"
  "v_mul_f32 v69, v69, v135\n"
  "v_mfma_f32_16x16x32_bf16 a[8:11], a[240:243], v[156:159], a[8:11]\n"
  "ds_read_b64_tr_b16 a[234:235], v223 offset:33792\n"
  "buffer_load_dwordx4 v242, s[40:43], s59 offen offset:1024 nt lds\n"
  "v_mul_f32 v70, v70, v135\n"
  "v_mul_f32 v71, v71, v135\n"
  "v_cvt_pk_bf16_f32 v68, v68, v69\n"
  "v_cvt_pk_bf16_f32 v69, v70, v71\n"
  "v_mfma_f32_16x16x32_bf16 a[12:15], a[248:251], v[156:159], a[12:15]\n"
  "ds_read_b64_tr_b16 a[236:237], v231 offset:32768\n"
  "ds_write2st64_b64 v193, v[4:5], v[68:69] offset0:0 offset1:4\n"
  "v_mfma_f32_16x16x32_bf16 v[76:79], a[248:251], v[152:155], v[76:79]\n"
  "ds_read_b64_tr_b16 a[238:239], v231 offset:33792\n"
  "ds_read_b64 v[128:129], v191 offset:7168\n"
  "buffer_load_dwordx4 v242, s[40:43], s59 offen offset:2048 nt lds\n"
  "v_mfma_f32_16x16x32_bf16 v[76:79], a[252:255], v[164:167], v[76:79]\n"
  "ds_read_b64_tr_b16 a[240:241], v216 offset:33024\n"
  "v_mul_f32 v8, v8, v134\n"
  "v_mul_f32 v9, v9, v134\n"
  "v_mul_f32 v10, v10, v134\n"
  "v_mul_f32 v11, v11, v134\n"
  "v_mfma_f32_16x16x32_bf16 v[12:15], a[252:255], v[160:163], v[12:15]\n"
  "ds_read_b64_tr_b16 a[242:243], v216 offset:34048\n"
  "ds_write_b128 v191, a[8:11] offset:2048\n"
  "v_cvt_pk_bf16_f32 v8, v8, v9\n"
  "v_cvt_pk_bf16_f32 v9, v10, v11\n"
  "v_mul_f32 v72, v72, v135\n"
  "v_mul_f32 v73, v73, v135\n"
  "v_mfma_f32_16x16x32_bf16 v[12:15], a[248:251], v[148:151], v[12:15]\n"
  "buffer_load_dwordx4 v242, s[40:43], s59 offen offset:3072 nt lds\n"
  "v_mul_f32 v74, v74, v135\n"
  "v_mul_f32 v75, v75, v135\n"
  "v_cvt_pk_bf16_f32 v72, v72, v73\n"
  "v_cvt_pk_bf16_f32 v73, v74, v75\n"
  "s_add_u32 s59, s59, 4096\n"
  "s_add_u32 m0, s61, 4096\n"
  "v_mfma_f32_16x16x32_bf16 v[16:19], a[176:179], v[148:151], v[16:19]\n"
  "ds_read_b64_tr_b16 a[244:245], v224 offset:33024\n"
  "ds_write2st64_b64 v194, v[8:9], v[72:73] offset0:0 offset1:4\n"
  "v_mfma_f32_16x16x32_bf16 v[16:19], a[180:183], v[160:163], v[16:19]\n"
  "ds_read_b64_tr_b16 a[246:247], v224 offset:34048\n"
  "buffer_load_dwordx4 v242, s[40:43], s59 offen offset:0 nt lds\n"
  "v_mfma_f32_16x16x32_bf16 v[80:83], a[180:183], v[164:167], v[80:83]\n"
  "ds_read_b64_tr_b16 a[248:249], v217 offset:33024\n"
  "v_mul_f32 v12, v12, v134\n"
  "v_mul_f32 v13, v13, v134\n"
  "v_mul_f32 v14, v14, v134\n"
  "v_mul_f32 v15, v15, v134\n"
  "v_mfma_f32_16x16x32_bf16 v[80:83], a[176:179], v[152:155], v[80:83]\n"
  "ds_read_b64_tr_b16 a[250:251], v217 offset:34048\n"
  "ds_write_b128 v191, a[12:15] offset:3072\n"
  "v_cvt_pk_bf16_f32 v12, v12, v13\n"
  "v_cvt_pk_bf16_f32 v13, v14, v15\n"
  "v_mul_f32 v76, v76, v135\n"
  "v_mul_f32 v77, v77, v135\n"
  "v_mfma_f32_16x16x32_bf16 a[16:19], a[176:179], v[156:159], a[16:19]\n"
  "ds_read_b64_tr_b16 a[252:253], v225 offset:33024\n"
  "buffer_load_dwordx4 v242, s[40:43], s59 offen offset:1024 nt lds\n"
  "v_mul_f32 v78, v78, v135\n"
  "v_mul_f32 v79, v79, v135\n"
  "v_cvt_pk_bf16_f32 v76, v76, v77\n"
  "v_cvt_pk_bf16_f32 v77, v78, v79\n"
  "v_mfma_f32_16x16x32_bf16 a[20:23], a[184:187], v[156:159], a[20:23]\n"
  "ds_read_b64_tr_b16 a[254:255], v225 offset:34048\n"
  "ds_write2st64_b64 v195, v[12:13], v[76:77] offset0:0 offset1:4\n"
  "v_mfma_f32_16x16x32_bf16 v[84:87], a[184:187], v[152:155], v[84:87]\n"
  "ds_read_b64_tr_b16 a[176:177], v218 offset:33024\n"
  "ds_read_b128 v[0:3], v196 offset:0\n"
  "ds_read_b128 v[4:7], v196 offset:1024\n"
  "buffer_load_dwordx4 v242, s[40:43], s59 offen offset:2048 nt lds\n"
  "v_mfma_f32_16x16x32_bf16 v[84:87], a[188:191], v[164:167], v[84:87]\n"
  "ds_read_b128 v[64:67], v196 offset:2048\n"
  "ds_read_b128 v[68:71], v196 offset:3072\n"
  "v_mul_f32 v16, v16, v134\n"
  "v_mul_f32 v17, v17, v134\n"
  "v_mul_f32 v18, v18, v134\n"
  "v_mul_f32 v19, v19, v134\n"
  "v_mfma_f32_16x16x32_bf16 v[20:23], a[188:191], v[160:163], v[20:23]\n"
  "ds_write_b128 v191, a[16:19] offset:4096\n"
  "v_cvt_pk_bf16_f32 v16, v16, v17\n"
  "v_cvt_pk_bf16_f32 v17, v18, v19\n"
  "v_mul_f32 v80, v80, v135\n"
  "v_mul_f32 v81, v81, v135\n"
  "v_mfma_f32_16x16x32_bf16 v[20:23], a[184:187], v[148:151], v[20:23]\n"
  "ds_read_b64_tr_b16 a[178:179], v218 offset:34048\n"
  "buffer_load_dwordx4 v242, s[40:43], s59 offen offset:3072 nt lds\n"
  "v_mul_f32 v82, v82, v135\n"
  "v_mul_f32 v83, v83, v135\n"
  "v_cvt_pk_bf16_f32 v80, v80, v81\n"
  "v_cvt_pk_bf16_f32 v81, v82, v83\n"
  "s_add_u32 s59, s59, 28672\n"
  "s_and_b32 s59, s59, 0x7ffff\n"
  "s_or_b32 s59, s59, s78\n"
  "v_mfma_f32_16x16x32_bf16 v[24:27], a[224:227], v[148:151], v[24:27]\n"
  "s_waitcnt lgkmcnt(13)\n"
  "ds_read_b64_tr_b16 a[180:181], v226 offset:33024\n"
  "ds_write2st64_b64 v192, v[16:17], v[80:81] offset0:0 offset1:4\n"
  "v_max_f32 v138, v130, v128\n"
  "v_sub_f32 v132, v130, v138\n"
  "v_sub_f32 v133, v128, v138\n"
  "v_exp_f32 v132, v132\n"
  "v_mfma_f32_16x16x32_bf16 v[24:27], a[228:231], v[160:163], v[24:27]\n"
  "ds_read_b64_tr_b16 a[182:183], v226 offset:34048\n"
  "s_waitcnt lgkmcnt(8)\n"
  "buffer_store_dwordx4 v[0:3], v188, s[52:55], 0 offen offset:0 nt\n"
  "v_exp_f32 v133, v133\n"
  "v_add_u32 v141, s94, v192\n"
  "v_add_u32 v142, s94, v193\n"
  "v_mul_f32 v136, v131, v132\n"
  "v_mfma_f32_16x16x32_bf16 v[88:91], a[228:231], v[164:167], v[88:91]\n"
  "ds_read_b64_tr_b16 a[184:185], v219 offset:33024\n"
  "v_fma_f32 v136, v129, v133, v136\n"
  "v_rcp_f32 v136, v136\n"
  "v_add_u32 v143, s94, v194\n"
  "v_add_u32 v144, s94, v195\n"
  "v_mfma_f32_16x16x32_bf16 v[88:91], a[224:227], v[152:155], v[88:91]\n"
  "ds_read_b64_tr_b16 a[186:187], v219 offset:34048\n"
  "ds_write_b128 v191, a[20:23] offset:5120\n"
  "v_add_u32 v145, s94, v196\n"
  "v_mul_f32 v132, v132, v136\n"
  "v_mul_f32 v133, v133, v136\n"
  "v_mul_f32 v20, v20, v134\n"
  "v_mfma_f32_16x16x32_bf16 a[24:27], a[224:227], v[156:159], a[24:27]\n"
  "ds_read_b64_tr_b16 a[188:189], v227 offset:33024\n"
  "s_waitcnt lgkmcnt(11)\n"
  "buffer_store_dwordx4 v[4:7], v188, s[52:55], s82 offen offset:0 nt\n"
  "v_mul_f32 v21, v21, v134\n"
  "v_mul_f32 v22, v22, v134\n"
  "v_mul_f32 v23, v23, v134\n"
  "v_cvt_pk_bf16_f32 v20, v20, v21\n"
  "v_mfma_f32_16x16x32_bf16 a[28:31], a[232:235], v[156:159], a[28:31]\n"
  "ds_read_b64_tr_b16 a[190:191], v227 offset:34048\n"
  "v_cvt_pk_bf16_f32 v21, v22, v23\n"
  "v_mul_f32 v84, v84, v135\n"
  "v_mul_f32 v85, v85, v135\n"
  "v_mul_f32 v86, v86, v135\n"
  "v_mfma_f32_16x16x32_bf16 v[92:95], a[232:235], v[152:155], v[92:95]\n"
  "ds_read_b64_tr_b16 a[224:225], v220 offset:33024\n"
  "s_waitcnt lgkmcnt(12)\n"
  "buffer_store_dwordx4 v[64:67], v188, s[52:55], s81 offen offset:0 nt\n"
  "v_mul_f32 v87, v87, v135\n"
  "v_cvt_pk_bf16_f32 v84, v84, v85\n"
  "v_cvt_pk_bf16_f32 v85, v86, v87\n"
  "v_mfma_f32_16x16x32_bf16 v[92:95], a[236:239], v[164:167], v[92:95]\n"
  "ds_write2st64_b64 v193, v[20:21], v[84:85] offset0:0 offset1:4\n"
  "v_mul_f32 v24, v24, v134\n"
  "v_mul_f32 v25, v25, v134\n"
  "v_mul_f32 v26, v26, v134\n"
  "v_mul_f32 v27, v27, v134\n"
  "v_mfma_f32_16x16x32_bf16 v[28:31], a[236:239], v[160:163], v[28:31]\n"
  "ds_read_b64_tr_b16 a[226:227], v220 offset:34048\n"
  "ds_read_b64_tr_b16 a[228:229], v228 offset:33024\n"
  "ds_write_b128 v191, a[24:27] offset:6144\n"
  "v_cvt_pk_bf16_f32 v24, v24, v25\n"
  "v_cvt_pk_bf16_f32 v25, v26, v27\n"
  "v_mul_f32 v88, v88, v135\n"
  "v_mul_f32 v89, v89, v135\n"
  "v_mfma_f32_16x16x32_bf16 v[28:31], a[232:235], v[148:151], v[28:31]\n"
  "ds_read_b64_tr_b16 a[230:231], v228 offset:34048\n"
  "s_waitcnt lgkmcnt(15)\n"
  "buffer_store_dwordx4 v[68:71], v188, s[52:55], s92 offen offset:0 nt\n"
  "v_mul_f32 v90, v90, v135\n"
  "v_mul_f32 v91, v91, v135\n"
  "v_cvt_pk_bf16_f32 v88, v88, v89\n"
  "v_cvt_pk_bf16_f32 v89, v90, v91\n"
  "v_mfma_f32_16x16x32_bf16 v[32:35], a[240:243], v[148:151], v[32:35]\n"
  "ds_write2st64_b64 v194, v[24:25], v[88:89] offset0:0 offset1:4\n"
  "v_mfma_f32_16x16x32_bf16 v[32:35], a[244:247], v[160:163], v[32:35]\n"
  "ds_read_b64_tr_b16 a[232:233], v221 offset:33024\n"
  "v_mfma_f32_16x16x32_bf16 v[96:99], a[244:247], v[164:167], v[96:99]\n"
  "ds_read_b64_tr_b16 a[234:235], v221 offset:34048\n"
  "ds_read_b64_tr_b16 a[236:237], v229 offset:33024\n"
  "v_mul_f32 v28, v28, v134\n"
  "v_mul_f32 v29, v29, v134\n"
  "v_mul_f32 v30, v30, v134\n"
  "v_mul_f32 v31, v31, v134\n"
  "v_mfma_f32_16x16x32_bf16 v[96:99], a[240:243], v[152:155], v[96:99]\n"
  "ds_write_b128 v191, a[28:31] offset:7168\n"
  "v_cvt_pk_bf16_f32 v28, v28, v29\n"
  "v_cvt_pk_bf16_f32 v29, v30, v31\n"
  "v_mul_f32 v92, v92, v135\n"
  "v_mul_f32 v93, v93, v135\n"
  "v_mfma_f32_16x16x32_bf16 a[32:35], a[240:243], v[156:159], a[32:35]\n"
  "s_waitcnt lgkmcnt(11)\n"
  "ds_read_b64_tr_b16 a[238:239], v229 offset:34048\n"
  "v_mul_f32 v94, v94, v135\n"
  "v_mul_f32 v95, v95, v135\n"
  "v_cvt_pk_bf16_f32 v92, v92, v93\n"
  "v_cvt_pk_bf16_f32 v93, v94, v95\n"
  "v_mfma_f32_16x16x32_bf16 a[36:39], a[248:251], v[156:159], a[36:39]\n"
  "ds_read_b64_tr_b16 a[240:241], v222 offset:33024\n"
  "ds_write2st64_b64 v195, v[28:29], v[92:93] offset0:0 offset1:4\n"
  "v_mfma_f32_16x16x32_bf16 v[100:103], a[248:251], v[152:155], v[100:103]\n"
  "ds_read_b128 v[16:19], v196 offset:0\n"
  "ds_read_b128 v[20:23], v196 offset:1024\n"
  "ds_read_b128 v[80:83], v196 offset:2048\n"
  "v_mfma_f32_16x16x32_bf16 v[100:103], a[252:255], v[164:167], v[100:103]\n"
  "ds_read_b64_tr_b16 a[242:243], v222 offset:34048\n"
  "ds_read_b64_tr_b16 a[244:245], v230 offset:33024\n"
  "ds_read_b128 v[84:87], v196 offset:3072\n"
  "v_mul_f32 v32, v32, v134\n"
  "v_mul_f32 v33, v33, v134\n"
  "v_mul_f32 v34, v34, v134\n"
  "v_mul_f32 v35, v35, v134\n"
  "v_mfma_f32_16x16x32_bf16 v[36:39], a[252:255], v[160:163], v[36:39]\n"
  "v_cvt_pk_bf16_f32 v32, v32, v33\n"
  "v_cvt_pk_bf16_f32 v33, v34, v35\n"
  "v_mul_f32 v96, v96, v135\n"
  "v_mul_f32 v97, v97, v135\n"
  "v_mfma_f32_16x16x32_bf16 v[36:39], a[248:251], v[148:151], v[36:39]\n"
  "s_waitcnt vmcnt(12) lgkmcnt(0)\n"
  "ds_read_b64_tr_b16 a[246:247], v230 offset:34048\n"
  "v_mul_f32 v98, v98, v135\n"
  "v_mul_f32 v99, v99, v135\n"
  "v_cvt_pk_bf16_f32 v96, v96, v97\n"
  "v_cvt_pk_bf16_f32 v97, v98, v99\n"
  "v_mfma_f32_16x16x32_bf16 v[40:43], a[176:179], v[148:151], v[40:43]\n"
  "s_barrier\n"
  "ds_read_b64_tr_b16 a[248:249], v223 offset:33024\n"
  "ds_read_b64_tr_b16 a[250:251], v223 offset:34048\n"
  "ds_write2st64_b64 v192, v[32:33], v[96:97] offset0:0 offset1:4\n"
  "v_mfma_f32_16x16x32_bf16 v[40:43], a[180:183], v[160:163], v[40:43]\n"
  "ds_read_b128 v[8:11], v137 offset:0\n"
  "ds_read_b128 v[12:15], v137 offset:1024\n"
  "ds_read_b128 v[72:75], v137 offset:2048\n"
  "buffer_store_dwordx4 v[16:19], v188, s[52:55], 0 offen offset:128 nt\n"
  "v_mfma_f32_16x16x32_bf16 v[104:107], a[180:183], v[164:167], v[104:107]\n"
  "ds_read_b64_tr_b16 a[252:253], v231 offset:33024\n"
  "ds_read_b128 v[76:79], v137 offset:3072\n"
  "ds_read_b128 v[24:27], v137 offset:4096\n"
  "v_mul_f32 v36, v36, v134\n"
  "v_mul_f32 v37, v37, v134\n"
  "v_mul_f32 v38, v38, v134\n"
  "v_mul_f32 v39, v39, v134\n"
  "v_cvt_pk_bf16_f32 v36, v36, v37\n"
  "v_cvt_pk_bf16_f32 v37, v38, v39\n"
  "v_mfma_f32_16x16x32_bf16 v[104:107], a[176:179], v[152:155], v[104:107]\n"
  "ds_read_b128 v[28:31], v137 offset:5120\n"
  "ds_read_b128 v[88:91], v137 offset:6144\n"
  "ds_read_b128 v[92:95], v137 offset:7168\n"
  "buffer_store_dwordx4 v[20:23], v188, s[52:55], s82 offen offset:128 nt\n"
  "v_mul_f32 v100, v100, v135\n"
  "v_mul_f32 v101, v101, v135\n"
  "v_mul_f32 v102, v102, v135\n"
  "v_mul_f32 v103, v103, v135\n"
  "v_cvt_pk_bf16_f32 v100, v100, v101\n"
  "v_cvt_pk_bf16_f32 v101, v102, v103\n"
  "v_mfma_f32_16x16x32_bf16 a[40:43], a[176:179], v[156:159], a[40:43]\n"
  "ds_read_b128 a[100:103], v200 offset:32768\n"
  "ds_read_b128 a[96:99], v199 offset:32768\n"
  "ds_read_b64_tr_b16 a[254:255], v231 offset:34048\n"
  "ds_write2st64_b64 v193, v[36:37], v[100:101] offset0:0 offset1:4\n"
  "v_accvgpr_read_b32 v168, a32\n"
  "v_accvgpr_read_b32 v169, a33\n"
  "v_accvgpr_read_b32 v170, a34\n"
  "v_accvgpr_read_b32 v171, a35\n"
  "v_mul_f32 v168, v168, v132\n"
  "v_mul_f32 v169, v169, v132\n"
  "v_mfma_f32_16x16x32_bf16 a[44:47], a[184:187], v[156:159], a[44:47]\n"
  "ds_read_b128 a[112:115], v199 offset:33024\n"
  "ds_read_b128 a[108:111], v202 offset:32768\n"
  "ds_read_b128 a[104:107], v201 offset:32768\n"
  "buffer_store_dwordx4 v[80:83], v188, s[52:55], s81 offen offset:128 nt\n"
  "v_mul_f32 v170, v170, v132\n"
  "v_mul_f32 v171, v171, v132\n"
  "s_waitcnt lgkmcnt(15)\n"
  "v_fma_f32 v168, v8, v133, v168\n"
  "v_fma_f32 v169, v9, v133, v169\n"
  "v_fma_f32 v170, v10, v133, v170\n"
  "v_fma_f32 v171, v11, v133, v171\n"
  "v_mfma_f32_16x16x32_bf16 v[108:111], a[184:187], v[152:155], v[108:111]\n"
  "ds_read_b128 a[124:127], v202 offset:33024\n"
  "ds_read_b128 a[120:123], v201 offset:33024\n"
  "ds_read_b128 a[116:119], v200 offset:33024\n"
  "v_cvt_pk_bf16_f32 v168, v168, v169\n"
  "v_cvt_pk_bf16_f32 v169, v170, v171\n"
  "v_mfma_f32_16x16x32_bf16 v[108:111], a[188:191], v[164:167], v[108:111]\n"
  "ds_read_b128 a[136:139], v197 offset:2048\n"
  "ds_read_b128 a[132:135], v198 offset:0\n"
  "ds_read_b128 a[128:131], v197 offset:0\n"
  "ds_write_b64 v141, v[168:169] offset:0\n"
  "v_mul_f32 v40, v40, v134\n"
  "v_mul_f32 v41, v41, v134\n"
  "v_mul_f32 v42, v42, v134\n"
  "v_mul_f32 v43, v43, v134\n"
  "v_cvt_pk_bf16_f32 v40, v40, v41\n"
  "v_cvt_pk_bf16_f32 v41, v42, v43\n"
  "v_mfma_f32_16x16x32_bf16 v[44:47], a[188:191], v[160:163], v[44:47]\n"
  "ds_read_b128 a[148:151], v198 offset:4096\n"
  "ds_read_b128 a[144:147], v197 offset:4096\n"
  "ds_read_b128 a[140:143], v198 offset:2048\n"
  "buffer_store_dwordx4 v[84:87], v188, s[52:55], s92 offen offset:128 nt\n"
  "v_mul_f32 v104, v104, v135\n"
  "v_mul_f32 v105, v105, v135\n"
  "v_mul_f32 v106, v106, v135\n"
  "v_mul_f32 v107, v107, v135\n"
  "v_cvt_pk_bf16_f32 v104, v104, v105\n"
  "v_cvt_pk_bf16_f32 v105, v106, v107\n"
  "v_mfma_f32_16x16x32_bf16 v[44:47], a[184:187], v[148:151], v[44:47]\n"
  "ds_read_b128 a[156:159], v198 offset:6144\n"
  "ds_read_b128 a[152:155], v197 offset:6144\n"
  "ds_write2st64_b64 v194, v[40:41], v[104:105] offset0:0 offset1:4\n"
  "v_accvgpr_read_b32 v172, a36\n"
  "v_accvgpr_read_b32 v173, a37\n"
  "v_accvgpr_read_b32 v174, a38\n"
  "v_accvgpr_read_b32 v175, a39\n"
  "v_mul_f32 v172, v172, v132\n"
  "v_mul_f32 v173, v173, v132\n"
  "v_mfma_f32_16x16x32_bf16 v[48:51], a[224:227], v[148:151], v[48:51]\n"
  "v_mul_f32 v174, v174, v132\n"
  "v_mul_f32 v175, v175, v132\n"
  "s_waitcnt lgkmcnt(15)\n"
  "v_fma_f32 v172, v12, v133, v172\n"
  "v_fma_f32 v173, v13, v133, v173\n"
  "v_fma_f32 v174, v14, v133, v174\n"
  "v_fma_f32 v175, v15, v133, v175\n"
  "v_mfma_f32_16x16x32_bf16 v[48:51], a[228:231], v[160:163], v[48:51]\n"
  "v_cvt_pk_bf16_f32 v172, v172, v173\n"
  "v_cvt_pk_bf16_f32 v173, v174, v175\n"
  "v_mfma_f32_16x16x32_bf16 v[112:115], a[228:231], v[164:167], v[112:115]\n"
  "ds_write_b64 v142, v[172:173] offset:0\n"
  "v_mul_f32 v44, v44, v134\n"
  "v_mul_f32 v45, v45, v134\n"
  "v_mul_f32 v46, v46, v134\n"
  "v_mul_f32 v47, v47, v134\n"
  "v_cvt_pk_bf16_f32 v44, v44, v45\n"
  "v_cvt_pk_bf16_f32 v45, v46, v47\n"
  "v_mfma_f32_16x16x32_bf16 v[112:115], a[224:227], v[152:155], v[112:115]\n"
  "s_waitcnt lgkmcnt(0)\n"
  "v_mul_f32 v108, v108, v135\n"
  "v_mul_f32 v109, v109, v135\n"
  "v_mul_f32 v110, v110, v135\n"
  "v_mul_f32 v111, v111, v135\n"
  "v_cvt_pk_bf16_f32 v108, v108, v109\n"
  "v_cvt_pk_bf16_f32 v109, v110, v111\n"
  "v_mfma_f32_16x16x32_bf16 a[48:51], a[224:227], v[156:159], a[48:51]\n"
  "s_barrier\n"
  "ds_write2st64_b64 v195, v[44:45], v[108:109] offset0:0 offset1:4\n"
  "v_accvgpr_read_b32 v176, a40\n"
  "v_accvgpr_read_b32 v177, a41\n"
  "v_accvgpr_read_b32 v178, a42\n"
  "v_accvgpr_read_b32 v179, a43\n"
  "v_mul_f32 v176, v176, v132\n"
  "v_mul_f32 v177, v177, v132\n"
  "s_add_u32 m0, s61, 32768\n"
  "v_mfma_f32_16x16x32_bf16 a[52:55], a[232:235], v[156:159], a[52:55]\n"
  "ds_read_b128 v[32:35], v196 offset:0\n"
  "ds_read_b128 v[36:39], v196 offset:1024\n"
  "ds_read_b128 v[96:99], v196 offset:2048\n"
  "buffer_load_dwordx4 v242, s[40:43], s59 offen offset:0 nt lds\n"
  "v_mul_f32 v178, v178, v132\n"
  "v_mul_f32 v179, v179, v132\n"
  "v_fma_f32 v176, v72, v133, v176\n"
  "v_fma_f32 v177, v73, v133, v177\n"
  "v_fma_f32 v178, v74, v133, v178\n"
  "v_fma_f32 v179, v75, v133, v179\n"
  "v_mfma_f32_16x16x32_bf16 v[116:119], a[232:235], v[152:155], v[116:119]\n"
  "ds_read_b128 v[100:103], v196 offset:3072\n"
  "v_cvt_pk_bf16_f32 v176, v176, v177\n"
  "v_cvt_pk_bf16_f32 v177, v178, v179\n"
  "v_mfma_f32_16x16x32_bf16 v[116:119], a[236:239], v[164:167], v[116:119]\n"
  "ds_write_b64 v143, v[176:177] offset:0\n"
  "v_mul_f32 v48, v48, v134\n"
  "v_mul_f32 v49, v49, v134\n"
  "v_mul_f32 v50, v50, v134\n"
  "v_mul_f32 v51, v51, v134\n"
  "v_cvt_pk_bf16_f32 v48, v48, v49\n"
  "v_cvt_pk_bf16_f32 v49, v50, v51\n"
  "v_mfma_f32_16x16x32_bf16 v[52:55], a[236:239], v[160:163], v[52:55]\n"
  "buffer_load_dwordx4 v242, s[40:43], s59 offen offset:1024 nt lds\n"
  "v_mul_f32 v112, v112, v135\n"
  "v_mul_f32 v113, v113, v135\n"
  "v_mul_f32 v114, v114, v135\n"
  "v_mul_f32 v115, v115, v135\n"
  "v_cvt_pk_bf16_f32 v112, v112, v113\n"
  "v_cvt_pk_bf16_f32 v113, v114, v115\n"
  "v_mfma_f32_16x16x32_bf16 v[52:55], a[232:235], v[148:151], v[52:55]\n"
  "ds_write2st64_b64 v192, v[48:49], v[112:113] offset0:0 offset1:4\n"
  "v_accvgpr_read_b32 v180, a44\n"
  "v_accvgpr_read_b32 v181, a45\n"
  "v_accvgpr_read_b32 v182, a46\n"
  "v_accvgpr_read_b32 v183, a47\n"
  "v_mul_f32 v180, v180, v132\n"
  "v_mul_f32 v181, v181, v132\n"
  "v_mfma_f32_16x16x32_bf16 v[56:59], a[240:243], v[148:151], v[56:59]\n"
  "s_waitcnt lgkmcnt(5)\n"
  "buffer_store_dwordx4 v[32:35], v188, s[52:55], 0 offen offset:256 nt\n"
  "v_mul_f32 v182, v182, v132\n"
  "v_mul_f32 v183, v183, v132\n"
  "v_fma_f32 v180, v76, v133, v180\n"
  "v_fma_f32 v181, v77, v133, v181\n"
  "v_fma_f32 v182, v78, v133, v182\n"
  "v_fma_f32 v183, v79, v133, v183\n"
  "v_mfma_f32_16x16x32_bf16 v[56:59], a[244:247], v[160:163], v[56:59]\n"
  "v_cvt_pk_bf16_f32 v180, v180, v181\n"
  "v_cvt_pk_bf16_f32 v181, v182, v183\n"
  "v_mfma_f32_16x16x32_bf16 v[120:123], a[244:247], v[164:167], v[120:123]\n"
  "ds_write_b64 v144, v[180:181] offset:0\n"
  "v_mul_f32 v52, v52, v134\n"
  "v_mul_f32 v53, v53, v134\n"
  "v_mul_f32 v54, v54, v134\n"
  "v_mul_f32 v55, v55, v134\n"
  "v_cvt_pk_bf16_f32 v52, v52, v53\n"
  "v_cvt_pk_bf16_f32 v53, v54, v55\n"
  "v_mfma_f32_16x16x32_bf16 v[120:123], a[240:243], v[152:155], v[120:123]\n"
  "ds_read_b128 v[244:247], v145 offset:0\n"
  "ds_read_b128 v[248:251], v145 offset:1024\n"
  "s_waitcnt lgkmcnt(7)\n"
  "buffer_store_dwordx4 v[36:39], v188, s[52:55], s82 offen offset:256 nt\n"
  "v_mul_f32 v116, v116, v135\n"
  "v_mul_f32 v117, v117, v135\n"
  "v_mul_f32 v118, v118, v135\n"
  "v_mul_f32 v119, v119, v135\n"
  "v_cvt_pk_bf16_f32 v116, v116, v117\n"
  "v_cvt_pk_bf16_f32 v117, v118, v119\n"
  "v_mfma_f32_16x16x32_bf16 a[56:59], a[240:243], v[156:159], a[56:59]\n"
  "ds_write2st64_b64 v193, v[52:53], v[116:117] offset0:0 offset1:4\n"
  "v_accvgpr_read_b32 v168, a48\n"
  "v_accvgpr_read_b32 v169, a49\n"
  "v_accvgpr_read_b32 v170, a50\n"
  "v_accvgpr_read_b32 v171, a51\n"
  "v_mul_f32 v168, v168, v132\n"
  "v_mul_f32 v169, v169, v132\n"
  "v_mfma_f32_16x16x32_bf16 a[60:63], a[248:251], v[156:159], a[60:63]\n"
  "s_waitcnt lgkmcnt(7)\n"
  "buffer_store_dwordx4 v[96:99], v188, s[52:55], s81 offen offset:256 nt\n"
  "v_mul_f32 v170, v170, v132\n"
  "v_mul_f32 v171, v171, v132\n"
  "v_fma_f32 v168, v24, v133, v168\n"
  "v_fma_f32 v169, v25, v133, v169\n"
  "v_fma_f32 v170, v26, v133, v170\n"
  "v_fma_f32 v171, v27, v133, v171\n"
  "v_mfma_f32_16x16x32_bf16 v[124:127], a[248:251], v[152:155], v[124:127]\n"
  "v_cvt_pk_bf16_f32 v168, v168, v169\n"
  "v_cvt_pk_bf16_f32 v169, v170, v171\n"
  "v_mfma_f32_16x16x32_bf16 v[124:127], a[252:255], v[164:167], v[124:127]\n"
  "ds_write_b64 v141, v[168:169] offset:0\n"
  "v_mul_f32 v56, v56, v134\n"
  "v_mul_f32 v57, v57, v134\n"
  "v_mul_f32 v58, v58, v134\n"
  "v_mul_f32 v59, v59, v134\n"
  "v_cvt_pk_bf16_f32 v56, v56, v57\n"
  "v_cvt_pk_bf16_f32 v57, v58, v59\n"
  "v_mfma_f32_16x16x32_bf16 v[60:63], a[252:255], v[160:163], v[60:63]\n"
  "s_waitcnt lgkmcnt(7)\n"
  "buffer_store_dwordx4 v[100:103], v188, s[52:55], s92 offen offset:256 nt\n"
  "v_mul_f32 v120, v120, v135\n"
  "v_mul_f32 v121, v121, v135\n"
  "v_mul_f32 v122, v122, v135\n"
  "v_mul_f32 v123, v123, v135\n"
  "v_cvt_pk_bf16_f32 v120, v120, v121\n"
  "v_cvt_pk_bf16_f32 v121, v122, v123\n"
  "v_mfma_f32_16x16x32_bf16 v[60:63], a[248:251], v[148:151], v[60:63]\n"
  "ds_write2st64_b64 v194, v[56:57], v[120:121] offset0:0 offset1:4\n"
  "v_accvgpr_read_b32 v172, a52\n"
  "v_accvgpr_read_b32 v173, a53\n"
  "v_accvgpr_read_b32 v174, a54\n"
  "v_accvgpr_read_b32 v175, a55\n"
  "v_mul_f32 v172, v172, v132\n"
  "v_mul_f32 v173, v173, v132\n"
  "s_waitcnt vmcnt(14)\n"
  "s_barrier\n"
  "ds_read_b128 a[160:163], v208 offset:0\n"
  "ds_read_b128 a[164:167], v209 offset:0\n"
  "ds_read_b128 a[168:171], v210 offset:0\n"
  "ds_read_b128 a[172:175], v211 offset:0\n"
  "ds_read_b128 a[176:179], v208 offset:256\n"
  "ds_read_b128 a[180:183], v209 offset:256\n"
  "ds_read_b128 a[184:187], v210 offset:256\n"
  "ds_read_b128 a[188:191], v211 offset:256\n"
  "s_waitcnt lgkmcnt(12)\n"
  "buffer_store_dwordx4 v[244:247], v189, s[52:55], 0 offen offset:0 nt\n"
  "v_mul_f32 v174, v174, v132\n"
  "v_mul_f32 v175, v175, v132\n"
  "v_fma_f32 v172, v28, v133, v172\n"
  "v_fma_f32 v173, v29, v133, v173\n"
  "v_fma_f32 v174, v30, v133, v174\n"
  "v_fma_f32 v175, v31, v133, v175\n"
  "v_cvt_pk_bf16_f32 v172, v172, v173\n"
  "v_cvt_pk_bf16_f32 v173, v174, v175\n"
  "ds_write_b64 v142, v[172:173] offset:0\n"
  "v_mul_f32 v60, v60, v134\n"
  "v_mul_f32 v61, v61, v134\n"
  "v_mul_f32 v62, v62, v134\n"
  "v_mul_f32 v63, v63, v134\n"
  "v_cvt_pk_bf16_f32 v60, v60, v61\n"
  "v_cvt_pk_bf16_f32 v61, v62, v63\n"
  "s_waitcnt lgkmcnt(12)\n"
  "buffer_store_dwordx4 v[248:251], v189, s[52:55], s82 offen offset:0 nt\n"
  "v_mul_f32 v124, v124, v135\n"
  "v_mul_f32 v125, v125, v135\n"
  "v_mul_f32 v126, v126, v135\n"
  "v_mul_f32 v127, v127, v135\n"
  "v_cvt_pk_bf16_f32 v124, v124, v125\n"
  "v_cvt_pk_bf16_f32 v125, v126, v127\n"
  "ds_write2st64_b64 v195, v[60:61], v[124:125] offset0:0 offset1:4\n"
  "v_accvgpr_read_b32 v176, a56\n"
  "v_accvgpr_read_b32 v177, a57\n"
  "v_accvgpr_read_b32 v178, a58\n"
  "v_accvgpr_read_b32 v179, a59\n"
  "v_mul_f32 v176, v176, v132\n"
  "v_mul_f32 v177, v177, v132\n"
  "ds_read_b128 v[48:51], v196 offset:0\n"
  "ds_read_b128 v[52:55], v196 offset:1024\n"
  "ds_read_b128 v[112:115], v196 offset:2048\n"
  "buffer_load_dwordx4 v242, s[40:43], s59 offen offset:2048 nt lds\n"
  "v_mul_f32 v178, v178, v132\n"
  "v_mul_f32 v179, v179, v132\n"
  "v_fma_f32 v176, v88, v133, v176\n"
  "v_fma_f32 v177, v89, v133, v177\n"
  "v_fma_f32 v178, v90, v133, v178\n"
  "v_fma_f32 v179, v91, v133, v179\n"
  "ds_read_b128 v[116:119], v196 offset:3072\n"
  "v_cvt_pk_bf16_f32 v176, v176, v177\n"
  "v_cvt_pk_bf16_f32 v177, v178, v179\n"
  "v_accvgpr_read_b32 v180, a60\n"
  "v_accvgpr_read_b32 v181, a61\n"
  "v_accvgpr_read_b32 v182, a62\n"
  "v_accvgpr_read_b32 v183, a63\n"
  "ds_write_b64 v143, v[176:177] offset:0\n"
  "v_mul_f32 v180, v180, v132\n"
  "v_mul_f32 v181, v181, v132\n"
  "v_mul_f32 v182, v182, v132\n"
  "v_mul_f32 v183, v183, v132\n"
  "v_fma_f32 v180, v92, v133, v180\n"
  "v_fma_f32 v181, v93, v133, v181\n"
  "buffer_load_dwordx4 v242, s[40:43], s59 offen offset:3072 nt lds\n"
  "v_fma_f32 v182, v94, v133, v182\n"
  "v_fma_f32 v183, v95, v133, v183\n"
  "v_cvt_pk_bf16_f32 v180, v180, v181\n"
  "v_cvt_pk_bf16_f32 v181, v182, v183\n"
  "s_add_u32 s59, s59, 4096\n"
  "s_add_u32 m0, s61, 36864\n"
  "ds_write_b64 v144, v[180:181] offset:0\n"
  "ds_read_b128 v[244:247], v145 offset:0\n"
  "ds_read_b128 v[248:251], v145 offset:1024\n"
  "s_waitcnt lgkmcnt(7)\n"
  "buffer_store_dwordx4 v[48:51], v188, s[52:55], 0 offen offset:384 nt\n"
  "s_waitcnt lgkmcnt(6)\n"
  "buffer_store_dwordx4 v[52:55], v188, s[52:55], s82 offen offset:384 nt\n"
  "s_waitcnt lgkmcnt(5)\n"
  "buffer_store_dwordx4 v[112:115], v188, s[52:55], s81 offen offset:384 nt\n"
  "s_waitcnt lgkmcnt(4)\n"
  "buffer_store_dwordx4 v[116:119], v188, s[52:55], s92 offen offset:384 nt\n"
  "s_waitcnt lgkmcnt(1)\n"
  "buffer_store_dwordx4 v[244:247], v189, s[52:55], 0 offen offset:128 nt\n"
  "s_waitcnt lgkmcnt(0)\n"
  "buffer_store_dwordx4 v[248:251], v189, s[52:55], s82 offen offset:128 nt\n"
  "buffer_load_dwordx4 v242, s[40:43], s59 offen offset:0 nt lds\n"
  "buffer_load_dwordx4 v242, s[40:43], s59 offen offset:1024 nt lds\n"
  "buffer_load_dwordx4 v242, s[40:43], s59 offen offset:2048 nt lds\n"
  "buffer_load_dwordx4 v242, s[40:43], s59 offen offset:3072 nt lds\n"
  "s_add_u32 s59, s59, 28672\n"
  "s_and_b32 s59, s59, 0x7ffff\n"
  "s_or_b32 s59, s59, s78\n"
  "s_branch .Lattn_FEND\n"
  ".Lattn_FINAL0:\n"
  "ds_read_b64_tr_b16 a[224:225], v216 offset:33024\n"
  "ds_read_b64_tr_b16 a[226:227], v216 offset:34048\n"
  "ds_read_b64_tr_b16 a[228:229], v224 offset:33024\n"
  "ds_read_b64_tr_b16 a[230:231], v224 offset:34048\n"
  "ds_read_b64_tr_b16 a[232:233], v217 offset:33024\n"
  "ds_read_b64_tr_b16 a[234:235], v217 offset:34048\n"
  "ds_read_b64_tr_b16 a[236:237], v225 offset:33024\n"
  "ds_read_b64_tr_b16 a[238:239], v225 offset:34048\n"
  "ds_read_b64_tr_b16 a[240:241], v218 offset:33024\n"
  "ds_read_b64_tr_b16 a[242:243], v218 offset:34048\n"
  "ds_read_b64_tr_b16 a[244:245], v226 offset:33024\n"
  "ds_read_b64_tr_b16 a[246:247], v226 offset:34048\n"
  "ds_read_b64_tr_b16 a[248:249], v219 offset:33024\n"
  "ds_read_b64_tr_b16 a[250:251], v219 offset:34048\n"
  "ds_read_b64_tr_b16 a[252:253], v227 offset:33024\n"
  "ds_read_b64_tr_b16 a[254:255], v227 offset:34048\n"
  "ds_read_b64_tr_b16 a[176:177], v220 offset:33024\n"
  "ds_read_b64_tr_b16 a[178:179], v220 offset:34048\n"
  "v_cvt_pk_bf16_f32 v164, v164, v165\n"
  "v_cvt_pk_bf16_f32 v165, v166, v167\n"
  "v_cvt_pk_bf16_f32 v166, v184, v185\n"
  "v_cvt_pk_bf16_f32 v167, v186, v187\n"
  "v_cvt_pk_bf16_f32 v162, v180, v181\n"
  "v_cvt_pk_bf16_f32 v163, v182, v183\n"
  "v_add_f32 v235, v235, v238\n"
  "v_add_f32 v236, v236, v239\n"
  "v_add_f32 v237, v237, v240\n"
  "v_mov_b32 v138, v235\n"
  "v_mov_b32 v139, v236\n"
  "v_mov_b32 v140, v237\n"
  "s_waitcnt lgkmcnt(0)\n"
  "v_mfma_f32_16x16x32_bf16 v[32:35], a[224:227], v[148:151], v[32:35]\n"
  "ds_read_b128 a[64:67], v199 offset:0\n"
  "ds_read_b128 a[68:71], v200 offset:0\n"
  "ds_read_b128 a[72:75], v201 offset:0\n"
  "v_permlane32_swap_b32 v235, v138\n"
  "v_permlane32_swap_b32 v236, v139\n"
  "v_permlane32_swap_b32 v237, v140\n"
  "v_add_f32 v235, v235, v138\n"
  "s_add_u32 m0, s93, 0\n"
  "v_mfma_f32_16x16x32_bf16 v[32:35], a[228:231], v[160:163], v[32:35]\n"
  "ds_read_b128 a[76:79], v202 offset:0\n"
  "ds_read_b128 a[80:83], v199 offset:256\n"
  "ds_read_b128 a[84:87], v200 offset:256\n"
  "buffer_load_dwordx4 v190, s[48:51], 0 offen offset:0 nt lds\n"
  "v_add_f32 v236, v236, v139\n"
  "v_add_f32 v237, v237, v140\n"
  "v_mov_b32 v138, v235\n"
  "v_mov_b32 v139, v236\n"
  "s_add_u32 m0, s93, 1024\n"
  "v_mfma_f32_16x16x32_bf16 v[96:99], a[228:231], v[164:167], v[96:99]\n"
  "ds_read_b64_tr_b16 a[180:181], v228 offset:33024\n"
  "ds_read_b128 a[88:91], v201 offset:256\n"
  "ds_read_b128 a[92:95], v202 offset:256\n"
  "v_mov_b32 v140, v237\n"
  "v_permlane16_swap_b32 v235, v138\n"
  "v_permlane16_swap_b32 v236, v139\n"
  "v_permlane16_swap_b32 v237, v140\n"
  "v_mfma_f32_16x16x32_bf16 v[96:99], a[224:227], v[152:155], v[96:99]\n"
  "ds_read_b64_tr_b16 a[182:183], v228 offset:34048\n"
  "buffer_load_dwordx4 v190, s[48:51], s82 offen offset:0 nt lds\n"
  "v_add_f32 v235, v235, v138\n"
  "v_add_f32 v236, v236, v139\n"
  "v_add_f32 v237, v237, v140\n"
  "v_mov_b32 v130, v234\n"
  "s_add_u32 m0, s93, 1920\n"
  "v_mfma_f32_16x16x32_bf16 a[32:35], a[224:227], v[156:159], a[32:35]\n"
  "ds_read_b64_tr_b16 a[184:185], v221 offset:33024\n"
  "v_mov_b32 v131, v237\n"
  "v_xor_b32 v137, 0x2000, v191\n"
  "v_rcp_f32 v134, v235\n"
  "v_rcp_f32 v135, v236\n"
  "v_mfma_f32_16x16x32_bf16 a[36:39], a[232:235], v[156:159], a[36:39]\n"
  "ds_read_b64_tr_b16 a[186:187], v221 offset:34048\n"
  "ds_write_b64 v137, v[130:131] offset:7168\n"
  "v_mfma_f32_16x16x32_bf16 v[100:103], a[232:235], v[152:155], v[100:103]\n"
  "ds_read_b64_tr_b16 a[188:189], v229 offset:33024\n"
  "buffer_load_dwordx4 v190, s[48:51], 0 offen offset:128 nt lds\n"
  "s_add_u32 m0, s93, 2944\n"
  "v_mfma_f32_16x16x32_bf16 v[100:103], a[236:239], v[164:167], v[100:103]\n"
  "ds_read_b64_tr_b16 a[190:191], v229 offset:34048\n"
  "v_mul_f32 v32, v32, v134\n"
  "v_mul_f32 v33, v33, v134\n"
  "v_mul_f32 v34, v34, v134\n"
  "v_mul_f32 v35, v35, v134\n"
  "v_mfma_f32_16x16x32_bf16 v[36:39], a[236:239], v[160:163], v[36:39]\n"
  "ds_read_b64_tr_b16 a[224:225], v222 offset:33024\n"
  "ds_write_b128 v191, a[32:35] offset:0\n"
  "v_cvt_pk_bf16_f32 v32, v32, v33\n"
  "v_cvt_pk_bf16_f32 v33, v34, v35\n"
  "v_mul_f32 v96, v96, v135\n"
  "v_mul_f32 v97, v97, v135\n"
  "v_mfma_f32_16x16x32_bf16 v[36:39], a[232:235], v[148:151], v[36:39]\n"
  "ds_read_b64_tr_b16 a[226:227], v222 offset:34048\n"
  "buffer_load_dwordx4 v190, s[48:51], s82 offen offset:128 nt lds\n"
  "v_mul_f32 v98, v98, v135\n"
  "v_mul_f32 v99, v99, v135\n"
  "v_cvt_pk_bf16_f32 v96, v96, v97\n"
  "v_cvt_pk_bf16_f32 v97, v98, v99\n"
  "v_mfma_f32_16x16x32_bf16 v[40:43], a[240:243], v[148:151], v[40:43]\n"
  "s_waitcnt lgkmcnt(0)\n"
  "ds_write2st64_b64 v192, v[32:33], v[96:97] offset0:0 offset1:4\n"
  "v_mfma_f32_16x16x32_bf16 v[40:43], a[244:247], v[160:163], v[40:43]\n"
  "s_barrier\n"
  "ds_read_b64_tr_b16 a[228:229], v230 offset:33024\n"
  "s_add_u32 m0, s61, 0\n"
  "v_mfma_f32_16x16x32_bf16 v[104:107], a[244:247], v[164:167], v[104:107]\n"
  "ds_read_b64_tr_b16 a[230:231], v230 offset:34048\n"
  "buffer_load_dwordx4 v242, s[40:43], s59 offen offset:0 nt lds\n"
  "v_mul_f32 v36, v36, v134\n"
  "v_mul_f32 v37, v37, v134\n"
  "v_mul_f32 v38, v38, v134\n"
  "v_mul_f32 v39, v39, v134\n"
  "v_mfma_f32_16x16x32_bf16 v[104:107], a[240:243], v[152:155], v[104:107]\n"
  "ds_read_b64_tr_b16 a[232:233], v223 offset:33024\n"
  "ds_write_b128 v191, a[36:39] offset:1024\n"
  "v_cvt_pk_bf16_f32 v36, v36, v37\n"
  "v_cvt_pk_bf16_f32 v37, v38, v39\n"
  "v_mul_f32 v100, v100, v135\n"
  "v_mul_f32 v101, v101, v135\n"
  "v_mfma_f32_16x16x32_bf16 a[40:43], a[240:243], v[156:159], a[40:43]\n"
  "ds_read_b64_tr_b16 a[234:235], v223 offset:34048\n"
  "buffer_load_dwordx4 v242, s[40:43], s59 offen offset:1024 nt lds\n"
  "v_mul_f32 v102, v102, v135\n"
  "v_mul_f32 v103, v103, v135\n"
  "v_cvt_pk_bf16_f32 v100, v100, v101\n"
  "v_cvt_pk_bf16_f32 v101, v102, v103\n"
  "v_mfma_f32_16x16x32_bf16 a[44:47], a[248:251], v[156:159], a[44:47]\n"
  "ds_read_b64_tr_b16 a[236:237], v231 offset:33024\n"
  "ds_write2st64_b64 v193, v[36:37], v[100:101] offset0:0 offset1:4\n"
  "v_mfma_f32_16x16x32_bf16 v[108:111], a[248:251], v[152:155], v[108:111]\n"
  "ds_read_b64_tr_b16 a[238:239], v231 offset:34048\n"
  "ds_read_b64 v[128:129], v191 offset:7168\n"
  "buffer_load_dwordx4 v242, s[40:43], s59 offen offset:2048 nt lds\n"
  "v_mfma_f32_16x16x32_bf16 v[108:111], a[252:255], v[164:167], v[108:111]\n"
  "ds_read_b64_tr_b16 a[240:241], v216 offset:32768\n"
  "v_mul_f32 v40, v40, v134\n"
  "v_mul_f32 v41, v41, v134\n"
  "v_mul_f32 v42, v42, v134\n"
  "v_mul_f32 v43, v43, v134\n"
  "v_mfma_f32_16x16x32_bf16 v[44:47], a[252:255], v[160:163], v[44:47]\n"
  "ds_read_b64_tr_b16 a[242:243], v216 offset:33792\n"
  "ds_write_b128 v191, a[40:43] offset:2048\n"
  "v_cvt_pk_bf16_f32 v40, v40, v41\n"
  "v_cvt_pk_bf16_f32 v41, v42, v43\n"
  "v_mul_f32 v104, v104, v135\n"
  "v_mul_f32 v105, v105, v135\n"
  "v_mfma_f32_16x16x32_bf16 v[44:47], a[248:251], v[148:151], v[44:47]\n"
  "buffer_load_dwordx4 v242, s[40:43], s59 offen offset:3072 nt lds\n"
  "v_mul_f32 v106, v106, v135\n"
  "v_mul_f32 v107, v107, v135\n"
  "v_cvt_pk_bf16_f32 v104, v104, v105\n"
  "v_cvt_pk_bf16_f32 v105, v106, v107\n"
  "s_add_u32 s59, s59, 4096\n"
  "s_add_u32 m0, s61, 4096\n"
  "v_mfma_f32_16x16x32_bf16 v[48:51], a[176:179], v[148:151], v[48:51]\n"
  "ds_read_b64_tr_b16 a[244:245], v224 offset:32768\n"
  "ds_write2st64_b64 v194, v[40:41], v[104:105] offset0:0 offset1:4\n"
  "v_mfma_f32_16x16x32_bf16 v[48:51], a[180:183], v[160:163], v[48:51]\n"
  "ds_read_b64_tr_b16 a[246:247], v224 offset:33792\n"
  "buffer_load_dwordx4 v242, s[40:43], s59 offen offset:0 nt lds\n"
  "v_mfma_f32_16x16x32_bf16 v[112:115], a[180:183], v[164:167], v[112:115]\n"
  "ds_read_b64_tr_b16 a[248:249], v217 offset:32768\n"
  "v_mul_f32 v44, v44, v134\n"
  "v_mul_f32 v45, v45, v134\n"
  "v_mul_f32 v46, v46, v134\n"
  "v_mul_f32 v47, v47, v134\n"
  "v_mfma_f32_16x16x32_bf16 v[112:115], a[176:179], v[152:155], v[112:115]\n"
  "ds_read_b64_tr_b16 a[250:251], v217 offset:33792\n"
  "ds_write_b128 v191, a[44:47] offset:3072\n"
  "v_cvt_pk_bf16_f32 v44, v44, v45\n"
  "v_cvt_pk_bf16_f32 v45, v46, v47\n"
  "v_mul_f32 v108, v108, v135\n"
  "v_mul_f32 v109, v109, v135\n"
  "v_mfma_f32_16x16x32_bf16 a[48:51], a[176:179], v[156:159], a[48:51]\n"
  "ds_read_b64_tr_b16 a[252:253], v225 offset:32768\n"
  "buffer_load_dwordx4 v242, s[40:43], s59 offen offset:1024 nt lds\n"
  "v_mul_f32 v110, v110, v135\n"
  "v_mul_f32 v111, v111, v135\n"
  "v_cvt_pk_bf16_f32 v108, v108, v109\n"
  "v_cvt_pk_bf16_f32 v109, v110, v111\n"
  "v_mfma_f32_16x16x32_bf16 a[52:55], a[184:187], v[156:159], a[52:55]\n"
  "ds_read_b64_tr_b16 a[254:255], v225 offset:33792\n"
  "ds_write2st64_b64 v195, v[44:45], v[108:109] offset0:0 offset1:4\n"
  "v_mfma_f32_16x16x32_bf16 v[116:119], a[184:187], v[152:155], v[116:119]\n"
  "ds_read_b64_tr_b16 a[176:177], v218 offset:32768\n"
  "ds_read_b128 v[32:35], v196 offset:0\n"
  "ds_read_b128 v[36:39], v196 offset:1024\n"
  "buffer_load_dwordx4 v242, s[40:43], s59 offen offset:2048 nt lds\n"
  "v_mfma_f32_16x16x32_bf16 v[116:119], a[188:191], v[164:167], v[116:119]\n"
  "ds_read_b128 v[96:99], v196 offset:2048\n"
  "ds_read_b128 v[100:103], v196 offset:3072\n"
  "v_mul_f32 v48, v48, v134\n"
  "v_mul_f32 v49, v49, v134\n"
  "v_mul_f32 v50, v50, v134\n"
  "v_mul_f32 v51, v51, v134\n"
  "v_mfma_f32_16x16x32_bf16 v[52:55], a[188:191], v[160:163], v[52:55]\n"
  "ds_write_b128 v191, a[48:51] offset:4096\n"
  "v_cvt_pk_bf16_f32 v48, v48, v49\n"
  "v_cvt_pk_bf16_f32 v49, v50, v51\n"
  "v_mul_f32 v112, v112, v135\n"
  "v_mul_f32 v113, v113, v135\n"
  "v_mfma_f32_16x16x32_bf16 v[52:55], a[184:187], v[148:151], v[52:55]\n"
  "ds_read_b64_tr_b16 a[178:179], v218 offset:33792\n"
  "buffer_load_dwordx4 v242, s[40:43], s59 offen offset:3072 nt lds\n"
  "v_mul_f32 v114, v114, v135\n"
  "v_mul_f32 v115, v115, v135\n"
  "v_cvt_pk_bf16_f32 v112, v112, v113\n"
  "v_cvt_pk_bf16_f32 v113, v114, v115\n"
  "s_add_u32 s59, s59, 28672\n"
  "s_and_b32 s59, s59, 0x7ffff\n"
  "s_or_b32 s59, s59, s78\n"
  "v_mfma_f32_16x16x32_bf16 v[56:59], a[224:227], v[148:151], v[56:59]\n"
  "s_waitcnt lgkmcnt(13)\n"
  "ds_read_b64_tr_b16 a[180:181], v226 offset:32768\n"
  "ds_write2st64_b64 v192, v[48:49], v[112:113] offset0:0 offset1:4\n"
  "v_max_f32 v138, v130, v128\n"
  "v_sub_f32 v132, v130, v138\n"
  "v_sub_f32 v133, v128, v138\n"
  "v_exp_f32 v132, v132\n"
  "v_mfma_f32_16x16x32_bf16 v[56:59], a[228:231], v[160:163], v[56:59]\n"
  "ds_read_b64_tr_b16 a[182:183], v226 offset:33792\n"
  "s_waitcnt lgkmcnt(8)\n"
  "buffer_store_dwordx4 v[32:35], v188, s[52:55], 0 offen offset:256 nt\n"
  "v_exp_f32 v133, v133\n"
  "v_add_u32 v141, s94, v192\n"
  "v_add_u32 v142, s94, v193\n"
  "v_mul_f32 v136, v131, v132\n"
  "v_mfma_f32_16x16x32_bf16 v[120:123], a[228:231], v[164:167], v[120:123]\n"
  "ds_read_b64_tr_b16 a[184:185], v219 offset:32768\n"
  "v_fma_f32 v136, v129, v133, v136\n"
  "v_rcp_f32 v136, v136\n"
  "v_add_u32 v143, s94, v194\n"
  "v_add_u32 v144, s94, v195\n"
  "v_mfma_f32_16x16x32_bf16 v[120:123], a[224:227], v[152:155], v[120:123]\n"
  "ds_read_b64_tr_b16 a[186:187], v219 offset:33792\n"
  "ds_write_b128 v191, a[52:55] offset:5120\n"
  "v_add_u32 v145, s94, v196\n"
  "v_mul_f32 v132, v132, v136\n"
  "v_mul_f32 v133, v133, v136\n"
  "v_mul_f32 v52, v52, v134\n"
  "v_mfma_f32_16x16x32_bf16 a[56:59], a[224:227], v[156:159], a[56:59]\n"
  "ds_read_b64_tr_b16 a[188:189], v227 offset:32768\n"
  "s_waitcnt lgkmcnt(11)\n"
  "buffer_store_dwordx4 v[36:39], v188, s[52:55], s82 offen offset:256 nt\n"
  "v_mul_f32 v53, v53, v134\n"
  "v_mul_f32 v54, v54, v134\n"
  "v_mul_f32 v55, v55, v134\n"
  "v_cvt_pk_bf16_f32 v52, v52, v53\n"
  "v_mfma_f32_16x16x32_bf16 a[60:63], a[232:235], v[156:159], a[60:63]\n"
  "ds_read_b64_tr_b16 a[190:191], v227 offset:33792\n"
  "v_cvt_pk_bf16_f32 v53, v54, v55\n"
  "v_mul_f32 v116, v116, v135\n"
  "v_mul_f32 v117, v117, v135\n"
  "v_mul_f32 v118, v118, v135\n"
  "v_mfma_f32_16x16x32_bf16 v[124:127], a[232:235], v[152:155], v[124:127]\n"
  "ds_read_b64_tr_b16 a[224:225], v220 offset:32768\n"
  "s_waitcnt lgkmcnt(12)\n"
  "buffer_store_dwordx4 v[96:99], v188, s[52:55], s81 offen offset:256 nt\n"
  "v_mul_f32 v119, v119, v135\n"
  "v_cvt_pk_bf16_f32 v116, v116, v117\n"
  "v_cvt_pk_bf16_f32 v117, v118, v119\n"
  "v_mfma_f32_16x16x32_bf16 v[124:127], a[236:239], v[164:167], v[124:127]\n"
  "ds_write2st64_b64 v193, v[52:53], v[116:117] offset0:0 offset1:4\n"
  "v_mul_f32 v56, v56, v134\n"
  "v_mul_f32 v57, v57, v134\n"
  "v_mul_f32 v58, v58, v134\n"
  "v_mul_f32 v59, v59, v134\n"
  "v_mfma_f32_16x16x32_bf16 v[60:63], a[236:239], v[160:163], v[60:63]\n"
  "ds_read_b64_tr_b16 a[226:227], v220 offset:33792\n"
  "ds_read_b64_tr_b16 a[228:229], v228 offset:32768\n"
  "ds_write_b128 v191, a[56:59] offset:6144\n"
  "v_cvt_pk_bf16_f32 v56, v56, v57\n"
  "v_cvt_pk_bf16_f32 v57, v58, v59\n"
  "v_mul_f32 v120, v120, v135\n"
  "v_mul_f32 v121, v121, v135\n"
  "v_mfma_f32_16x16x32_bf16 v[60:63], a[232:235], v[148:151], v[60:63]\n"
  "ds_read_b64_tr_b16 a[230:231], v228 offset:33792\n"
  "s_waitcnt lgkmcnt(15)\n"
  "buffer_store_dwordx4 v[100:103], v188, s[52:55], s92 offen offset:256 nt\n"
  "v_mul_f32 v122, v122, v135\n"
  "v_mul_f32 v123, v123, v135\n"
  "v_cvt_pk_bf16_f32 v120, v120, v121\n"
  "v_cvt_pk_bf16_f32 v121, v122, v123\n"
  "v_mfma_f32_16x16x32_bf16 v[0:3], a[240:243], v[148:151], v[0:3]\n"
  "ds_write2st64_b64 v194, v[56:57], v[120:121] offset0:0 offset1:4\n"
  "v_mfma_f32_16x16x32_bf16 v[0:3], a[244:247], v[160:163], v[0:3]\n"
  "ds_read_b64_tr_b16 a[232:233], v221 offset:32768\n"
  "v_mfma_f32_16x16x32_bf16 v[64:67], a[244:247], v[164:167], v[64:67]\n"
  "ds_read_b64_tr_b16 a[234:235], v221 offset:33792\n"
  "ds_read_b64_tr_b16 a[236:237], v229 offset:32768\n"
  "v_mul_f32 v60, v60, v134\n"
  "v_mul_f32 v61, v61, v134\n"
  "v_mul_f32 v62, v62, v134\n"
  "v_mul_f32 v63, v63, v134\n"
  "v_mfma_f32_16x16x32_bf16 v[64:67], a[240:243], v[152:155], v[64:67]\n"
  "ds_write_b128 v191, a[60:63] offset:7168\n"
  "v_cvt_pk_bf16_f32 v60, v60, v61\n"
  "v_cvt_pk_bf16_f32 v61, v62, v63\n"
  "v_mul_f32 v124, v124, v135\n"
  "v_mul_f32 v125, v125, v135\n"
  "v_mfma_f32_16x16x32_bf16 a[0:3], a[240:243], v[156:159], a[0:3]\n"
  "s_waitcnt lgkmcnt(11)\n"
  "ds_read_b64_tr_b16 a[238:239], v229 offset:33792\n"
  "v_mul_f32 v126, v126, v135\n"
  "v_mul_f32 v127, v127, v135\n"
  "v_cvt_pk_bf16_f32 v124, v124, v125\n"
  "v_cvt_pk_bf16_f32 v125, v126, v127\n"
  "v_mfma_f32_16x16x32_bf16 a[4:7], a[248:251], v[156:159], a[4:7]\n"
  "ds_read_b64_tr_b16 a[240:241], v222 offset:32768\n"
  "ds_write2st64_b64 v195, v[60:61], v[124:125] offset0:0 offset1:4\n"
  "v_mfma_f32_16x16x32_bf16 v[68:71], a[248:251], v[152:155], v[68:71]\n"
  "ds_read_b128 v[48:51], v196 offset:0\n"
  "ds_read_b128 v[52:55], v196 offset:1024\n"
  "ds_read_b128 v[112:115], v196 offset:2048\n"
  "v_mfma_f32_16x16x32_bf16 v[68:71], a[252:255], v[164:167], v[68:71]\n"
  "ds_read_b64_tr_b16 a[242:243], v222 offset:33792\n"
  "ds_read_b64_tr_b16 a[244:245], v230 offset:32768\n"
  "ds_read_b128 v[116:119], v196 offset:3072\n"
  "v_mul_f32 v0, v0, v134\n"
  "v_mul_f32 v1, v1, v134\n"
  "v_mul_f32 v2, v2, v134\n"
  "v_mul_f32 v3, v3, v134\n"
  "v_mfma_f32_16x16x32_bf16 v[4:7], a[252:255], v[160:163], v[4:7]\n"
  "v_cvt_pk_bf16_f32 v0, v0, v1\n"
  "v_cvt_pk_bf16_f32 v1, v2, v3\n"
  "v_mul_f32 v64, v64, v135\n"
  "v_mul_f32 v65, v65, v135\n"
  "v_mfma_f32_16x16x32_bf16 v[4:7], a[248:251], v[148:151], v[4:7]\n"
  "s_waitcnt vmcnt(12) lgkmcnt(0)\n"
  "ds_read_b64_tr_b16 a[246:247], v230 offset:33792\n"
  "v_mul_f32 v66, v66, v135\n"
  "v_mul_f32 v67, v67, v135\n"
  "v_cvt_pk_bf16_f32 v64, v64, v65\n"
  "v_cvt_pk_bf16_f32 v65, v66, v67\n"
  "v_mfma_f32_16x16x32_bf16 v[8:11], a[176:179], v[148:151], v[8:11]\n"
  "s_barrier\n"
  "ds_read_b64_tr_b16 a[248:249], v223 offset:32768\n"
  "ds_read_b64_tr_b16 a[250:251], v223 offset:33792\n"
  "ds_write2st64_b64 v192, v[0:1], v[64:65] offset0:0 offset1:4\n"
  "v_mfma_f32_16x16x32_bf16 v[8:11], a[180:183], v[160:163], v[8:11]\n"
  "ds_read_b128 v[40:43], v137 offset:0\n"
  "ds_read_b128 v[44:47], v137 offset:1024\n"
  "ds_read_b128 v[104:107], v137 offset:2048\n"
  "buffer_store_dwordx4 v[48:51], v188, s[52:55], 0 offen offset:384 nt\n"
  "v_mfma_f32_16x16x32_bf16 v[72:75], a[180:183], v[164:167], v[72:75]\n"
  "ds_read_b64_tr_b16 a[252:253], v231 offset:32768\n"
  "ds_read_b128 v[108:111], v137 offset:3072\n"
  "ds_read_b128 v[56:59], v137 offset:4096\n"
  "v_mul_f32 v4, v4, v134\n"
  "v_mul_f32 v5, v5, v134\n"
  "v_mul_f32 v6, v6, v134\n"
  "v_mul_f32 v7, v7, v134\n"
  "v_cvt_pk_bf16_f32 v4, v4, v5\n"
  "v_cvt_pk_bf16_f32 v5, v6, v7\n"
  "v_mfma_f32_16x16x32_bf16 v[72:75], a[176:179], v[152:155], v[72:75]\n"
  "ds_read_b128 v[60:63], v137 offset:5120\n"
  "ds_read_b128 v[120:123], v137 offset:6144\n"
  "ds_read_b128 v[124:127], v137 offset:7168\n"
  "buffer_store_dwordx4 v[52:55], v188, s[52:55], s82 offen offset:384 nt\n"
  "v_mul_f32 v68, v68, v135\n"
  "v_mul_f32 v69, v69, v135\n"
  "v_mul_f32 v70, v70, v135\n"
  "v_mul_f32 v71, v71, v135\n"
  "v_cvt_pk_bf16_f32 v68, v68, v69\n"
  "v_cvt_pk_bf16_f32 v69, v70, v71\n"
  "v_mfma_f32_16x16x32_bf16 a[8:11], a[176:179], v[156:159], a[8:11]\n"
  "ds_read_b128 a[100:103], v200 offset:32768\n"
  "ds_read_b128 a[96:99], v199 offset:32768\n"
  "ds_read_b64_tr_b16 a[254:255], v231 offset:33792\n"
  "ds_write2st64_b64 v193, v[4:5], v[68:69] offset0:0 offset1:4\n"
  "v_accvgpr_read_b32 v168, a0\n"
  "v_accvgpr_read_b32 v169, a1\n"
  "v_accvgpr_read_b32 v170, a2\n"
  "v_accvgpr_read_b32 v171, a3\n"
  "v_mul_f32 v168, v168, v132\n"
  "v_mul_f32 v169, v169, v132\n"
  "v_mfma_f32_16x16x32_bf16 a[12:15], a[184:187], v[156:159], a[12:15]\n"
  "ds_read_b128 a[112:115], v199 offset:33024\n"
  "ds_read_b128 a[108:111], v202 offset:32768\n"
  "ds_read_b128 a[104:107], v201 offset:32768\n"
  "buffer_store_dwordx4 v[112:115], v188, s[52:55], s81 offen offset:384 nt\n"
  "v_mul_f32 v170, v170, v132\n"
  "v_mul_f32 v171, v171, v132\n"
  "s_waitcnt lgkmcnt(15)\n"
  "v_fma_f32 v168, v40, v133, v168\n"
  "v_fma_f32 v169, v41, v133, v169\n"
  "v_fma_f32 v170, v42, v133, v170\n"
  "v_fma_f32 v171, v43, v133, v171\n"
  "v_mfma_f32_16x16x32_bf16 v[76:79], a[184:187], v[152:155], v[76:79]\n"
  "ds_read_b128 a[124:127], v202 offset:33024\n"
  "ds_read_b128 a[120:123], v201 offset:33024\n"
  "ds_read_b128 a[116:119], v200 offset:33024\n"
  "v_cvt_pk_bf16_f32 v168, v168, v169\n"
  "v_cvt_pk_bf16_f32 v169, v170, v171\n"
  "v_mfma_f32_16x16x32_bf16 v[76:79], a[188:191], v[164:167], v[76:79]\n"
  "ds_read_b128 a[136:139], v197 offset:2048\n"
  "ds_read_b128 a[132:135], v198 offset:0\n"
  "ds_read_b128 a[128:131], v197 offset:0\n"
  "ds_write_b64 v141, v[168:169] offset:0\n"
  "v_mul_f32 v8, v8, v134\n"
  "v_mul_f32 v9, v9, v134\n"
  "v_mul_f32 v10, v10, v134\n"
  "v_mul_f32 v11, v11, v134\n"
  "v_cvt_pk_bf16_f32 v8, v8, v9\n"
  "v_cvt_pk_bf16_f32 v9, v10, v11\n"
  "v_mfma_f32_16x16x32_bf16 v[12:15], a[188:191], v[160:163], v[12:15]\n"
  "ds_read_b128 a[148:151], v198 offset:4096\n"
  "ds_read_b128 a[144:147], v197 offset:4096\n"
  "ds_read_b128 a[140:143], v198 offset:2048\n"
  "buffer_store_dwordx4 v[116:119], v188, s[52:55], s92 offen offset:384 nt\n"
  "v_mul_f32 v72, v72, v135\n"
  "v_mul_f32 v73, v73, v135\n"
  "v_mul_f32 v74, v74, v135\n"
  "v_mul_f32 v75, v75, v135\n"
  "v_cvt_pk_bf16_f32 v72, v72, v73\n"
  "v_cvt_pk_bf16_f32 v73, v74, v75\n"
  "v_mfma_f32_16x16x32_bf16 v[12:15], a[184:187], v[148:151], v[12:15]\n"
  "ds_read_b128 a[156:159], v198 offset:6144\n"
  "ds_read_b128 a[152:155], v197 offset:6144\n"
  "ds_write2st64_b64 v194, v[8:9], v[72:73] offset0:0 offset1:4\n"
  "v_accvgpr_read_b32 v172, a4\n"
  "v_accvgpr_read_b32 v173, a5\n"
  "v_accvgpr_read_b32 v174, a6\n"
  "v_accvgpr_read_b32 v175, a7\n"
  "v_mul_f32 v172, v172, v132\n"
  "v_mul_f32 v173, v173, v132\n"
  "v_mfma_f32_16x16x32_bf16 v[16:19], a[224:227], v[148:151], v[16:19]\n"
  "v_mul_f32 v174, v174, v132\n"
  "v_mul_f32 v175, v175, v132\n"
  "s_waitcnt lgkmcnt(15)\n"
  "v_fma_f32 v172, v44, v133, v172\n"
  "v_fma_f32 v173, v45, v133, v173\n"
  "v_fma_f32 v174, v46, v133, v174\n"
  "v_fma_f32 v175, v47, v133, v175\n"
  "v_mfma_f32_16x16x32_bf16 v[16:19], a[228:231], v[160:163], v[16:19]\n"
  "v_cvt_pk_bf16_f32 v172, v172, v173\n"
  "v_cvt_pk_bf16_f32 v173, v174, v175\n"
  "v_mfma_f32_16x16x32_bf16 v[80:83], a[228:231], v[164:167], v[80:83]\n"
  "ds_write_b64 v142, v[172:173] offset:0\n"
  "v_mul_f32 v12, v12, v134\n"
  "v_mul_f32 v13, v13, v134\n"
  "v_mul_f32 v14, v14, v134\n"
  "v_mul_f32 v15, v15, v134\n"
  "v_cvt_pk_bf16_f32 v12, v12, v13\n"
  "v_cvt_pk_bf16_f32 v13, v14, v15\n"
  "v_mfma_f32_16x16x32_bf16 v[80:83], a[224:227], v[152:155], v[80:83]\n"
  "s_waitcnt lgkmcnt(0)\n"
  "v_mul_f32 v76, v76, v135\n"
  "v_mul_f32 v77, v77, v135\n"
  "v_mul_f32 v78, v78, v135\n"
  "v_mul_f32 v79, v79, v135\n"
  "v_cvt_pk_bf16_f32 v76, v76, v77\n"
  "v_cvt_pk_bf16_f32 v77, v78, v79\n"
  "v_mfma_f32_16x16x32_bf16 a[16:19], a[224:227], v[156:159], a[16:19]\n"
  "s_barrier\n"
  "ds_write2st64_b64 v195, v[12:13], v[76:77] offset0:0 offset1:4\n"
  "v_accvgpr_read_b32 v176, a8\n"
  "v_accvgpr_read_b32 v177, a9\n"
  "v_accvgpr_read_b32 v178, a10\n"
  "v_accvgpr_read_b32 v179, a11\n"
  "v_mul_f32 v176, v176, v132\n"
  "v_mul_f32 v177, v177, v132\n"
  "s_add_u32 m0, s61, 32768\n"
  "v_mfma_f32_16x16x32_bf16 a[20:23], a[232:235], v[156:159], a[20:23]\n"
  "ds_read_b128 v[0:3], v196 offset:0\n"
  "ds_read_b128 v[4:7], v196 offset:1024\n"
  "ds_read_b128 v[64:67], v196 offset:2048\n"
  "buffer_load_dwordx4 v242, s[40:43], s59 offen offset:0 nt lds\n"
  "v_mul_f32 v178, v178, v132\n"
  "v_mul_f32 v179, v179, v132\n"
  "v_fma_f32 v176, v104, v133, v176\n"
  "v_fma_f32 v177, v105, v133, v177\n"
  "v_fma_f32 v178, v106, v133, v178\n"
  "v_fma_f32 v179, v107, v133, v179\n"
  "v_mfma_f32_16x16x32_bf16 v[84:87], a[232:235], v[152:155], v[84:87]\n"
  "ds_read_b128 v[68:71], v196 offset:3072\n"
  "v_cvt_pk_bf16_f32 v176, v176, v177\n"
  "v_cvt_pk_bf16_f32 v177, v178, v179\n"
  "v_mfma_f32_16x16x32_bf16 v[84:87], a[236:239], v[164:167], v[84:87]\n"
  "ds_write_b64 v143, v[176:177] offset:0\n"
  "v_mul_f32 v16, v16, v134\n"
  "v_mul_f32 v17, v17, v134\n"
  "v_mul_f32 v18, v18, v134\n"
  "v_mul_f32 v19, v19, v134\n"
  "v_cvt_pk_bf16_f32 v16, v16, v17\n"
  "v_cvt_pk_bf16_f32 v17, v18, v19\n"
  "v_mfma_f32_16x16x32_bf16 v[20:23], a[236:239], v[160:163], v[20:23]\n"
  "buffer_load_dwordx4 v242, s[40:43], s59 offen offset:1024 nt lds\n"
  "v_mul_f32 v80, v80, v135\n"
  "v_mul_f32 v81, v81, v135\n"
  "v_mul_f32 v82, v82, v135\n"
  "v_mul_f32 v83, v83, v135\n"
  "v_cvt_pk_bf16_f32 v80, v80, v81\n"
  "v_cvt_pk_bf16_f32 v81, v82, v83\n"
  "v_mfma_f32_16x16x32_bf16 v[20:23], a[232:235], v[148:151], v[20:23]\n"
  "ds_write2st64_b64 v192, v[16:17], v[80:81] offset0:0 offset1:4\n"
  "v_accvgpr_read_b32 v180, a12\n"
  "v_accvgpr_read_b32 v181, a13\n"
  "v_accvgpr_read_b32 v182, a14\n"
  "v_accvgpr_read_b32 v183, a15\n"
  "v_mul_f32 v180, v180, v132\n"
  "v_mul_f32 v181, v181, v132\n"
  "v_mfma_f32_16x16x32_bf16 v[24:27], a[240:243], v[148:151], v[24:27]\n"
  "s_waitcnt lgkmcnt(5)\n"
  "buffer_store_dwordx4 v[0:3], v188, s[52:55], 0 offen offset:0 nt\n"
  "v_mul_f32 v182, v182, v132\n"
  "v_mul_f32 v183, v183, v132\n"
  "v_fma_f32 v180, v108, v133, v180\n"
  "v_fma_f32 v181, v109, v133, v181\n"
  "v_fma_f32 v182, v110, v133, v182\n"
  "v_fma_f32 v183, v111, v133, v183\n"
  "v_mfma_f32_16x16x32_bf16 v[24:27], a[244:247], v[160:163], v[24:27]\n"
  "v_cvt_pk_bf16_f32 v180, v180, v181\n"
  "v_cvt_pk_bf16_f32 v181, v182, v183\n"
  "v_mfma_f32_16x16x32_bf16 v[88:91], a[244:247], v[164:167], v[88:91]\n"
  "ds_write_b64 v144, v[180:181] offset:0\n"
  "v_mul_f32 v20, v20, v134\n"
  "v_mul_f32 v21, v21, v134\n"
  "v_mul_f32 v22, v22, v134\n"
  "v_mul_f32 v23, v23, v134\n"
  "v_cvt_pk_bf16_f32 v20, v20, v21\n"
  "v_cvt_pk_bf16_f32 v21, v22, v23\n"
  "v_mfma_f32_16x16x32_bf16 v[88:91], a[240:243], v[152:155], v[88:91]\n"
  "ds_read_b128 v[244:247], v145 offset:0\n"
  "ds_read_b128 v[248:251], v145 offset:1024\n"
  "s_waitcnt lgkmcnt(7)\n"
  "buffer_store_dwordx4 v[4:7], v188, s[52:55], s82 offen offset:0 nt\n"
  "v_mul_f32 v84, v84, v135\n"
  "v_mul_f32 v85, v85, v135\n"
  "v_mul_f32 v86, v86, v135\n"
  "v_mul_f32 v87, v87, v135\n"
  "v_cvt_pk_bf16_f32 v84, v84, v85\n"
  "v_cvt_pk_bf16_f32 v85, v86, v87\n"
  "v_mfma_f32_16x16x32_bf16 a[24:27], a[240:243], v[156:159], a[24:27]\n"
  "ds_write2st64_b64 v193, v[20:21], v[84:85] offset0:0 offset1:4\n"
  "v_accvgpr_read_b32 v168, a16\n"
  "v_accvgpr_read_b32 v169, a17\n"
  "v_accvgpr_read_b32 v170, a18\n"
  "v_accvgpr_read_b32 v171, a19\n"
  "v_mul_f32 v168, v168, v132\n"
  "v_mul_f32 v169, v169, v132\n"
  "v_mfma_f32_16x16x32_bf16 a[28:31], a[248:251], v[156:159], a[28:31]\n"
  "s_waitcnt lgkmcnt(7)\n"
  "buffer_store_dwordx4 v[64:67], v188, s[52:55], s81 offen offset:0 nt\n"
  "v_mul_f32 v170, v170, v132\n"
  "v_mul_f32 v171, v171, v132\n"
  "v_fma_f32 v168, v56, v133, v168\n"
  "v_fma_f32 v169, v57, v133, v169\n"
  "v_fma_f32 v170, v58, v133, v170\n"
  "v_fma_f32 v171, v59, v133, v171\n"
  "v_mfma_f32_16x16x32_bf16 v[92:95], a[248:251], v[152:155], v[92:95]\n"
  "v_cvt_pk_bf16_f32 v168, v168, v169\n"
  "v_cvt_pk_bf16_f32 v169, v170, v171\n"
  "v_mfma_f32_16x16x32_bf16 v[92:95], a[252:255], v[164:167], v[92:95]\n"
  "ds_write_b64 v141, v[168:169] offset:0\n"
  "v_mul_f32 v24, v24, v134\n"
  "v_mul_f32 v25, v25, v134\n"
  "v_mul_f32 v26, v26, v134\n"
  "v_mul_f32 v27, v27, v134\n"
  "v_cvt_pk_bf16_f32 v24, v24, v25\n"
  "v_cvt_pk_bf16_f32 v25, v26, v27\n"
  "v_mfma_f32_16x16x32_bf16 v[28:31], a[252:255], v[160:163], v[28:31]\n"
  "s_waitcnt lgkmcnt(7)\n"
  "buffer_store_dwordx4 v[68:71], v188, s[52:55], s92 offen offset:0 nt\n"
  "v_mul_f32 v88, v88, v135\n"
  "v_mul_f32 v89, v89, v135\n"
  "v_mul_f32 v90, v90, v135\n"
  "v_mul_f32 v91, v91, v135\n"
  "v_cvt_pk_bf16_f32 v88, v88, v89\n"
  "v_cvt_pk_bf16_f32 v89, v90, v91\n"
  "v_mfma_f32_16x16x32_bf16 v[28:31], a[248:251], v[148:151], v[28:31]\n"
  "ds_write2st64_b64 v194, v[24:25], v[88:89] offset0:0 offset1:4\n"
  "v_accvgpr_read_b32 v172, a20\n"
  "v_accvgpr_read_b32 v173, a21\n"
  "v_accvgpr_read_b32 v174, a22\n"
  "v_accvgpr_read_b32 v175, a23\n"
  "v_mul_f32 v172, v172, v132\n"
  "v_mul_f32 v173, v173, v132\n"
  "s_waitcnt vmcnt(14)\n"
  "s_barrier\n"
  "ds_read_b128 a[160:163], v208 offset:0\n"
  "ds_read_b128 a[164:167], v209 offset:0\n"
  "ds_read_b128 a[168:171], v210 offset:0\n"
  "ds_read_b128 a[172:175], v211 offset:0\n"
  "ds_read_b128 a[176:179], v208 offset:256\n"
  "ds_read_b128 a[180:183], v209 offset:256\n"
  "ds_read_b128 a[184:187], v210 offset:256\n"
  "ds_read_b128 a[188:191], v211 offset:256\n"
  "s_waitcnt lgkmcnt(12)\n"
  "buffer_store_dwordx4 v[244:247], v189, s[52:55], 0 offen offset:0 nt\n"
  "v_mul_f32 v174, v174, v132\n"
  "v_mul_f32 v175, v175, v132\n"
  "v_fma_f32 v172, v60, v133, v172\n"
  "v_fma_f32 v173, v61, v133, v173\n"
  "v_fma_f32 v174, v62, v133, v174\n"
  "v_fma_f32 v175, v63, v133, v175\n"
  "v_cvt_pk_bf16_f32 v172, v172, v173\n"
  "v_cvt_pk_bf16_f32 v173, v174, v175\n"
  "ds_write_b64 v142, v[172:173] offset:0\n"
  "v_mul_f32 v28, v28, v134\n"
  "v_mul_f32 v29, v29, v134\n"
  "v_mul_f32 v30, v30, v134\n"
  "v_mul_f32 v31, v31, v134\n"
  "v_cvt_pk_bf16_f32 v28, v28, v29\n"
  "v_cvt_pk_bf16_f32 v29, v30, v31\n"
  "s_waitcnt lgkmcnt(12)\n"
  "buffer_store_dwordx4 v[248:251], v189, s[52:55], s82 offen offset:0 nt\n"
  "v_mul_f32 v92, v92, v135\n"
  "v_mul_f32 v93, v93, v135\n"
  "v_mul_f32 v94, v94, v135\n"
  "v_mul_f32 v95, v95, v135\n"
  "v_cvt_pk_bf16_f32 v92, v92, v93\n"
  "v_cvt_pk_bf16_f32 v93, v94, v95\n"
  "ds_write2st64_b64 v195, v[28:29], v[92:93] offset0:0 offset1:4\n"
  "v_accvgpr_read_b32 v176, a24\n"
  "v_accvgpr_read_b32 v177, a25\n"
  "v_accvgpr_read_b32 v178, a26\n"
  "v_accvgpr_read_b32 v179, a27\n"
  "v_mul_f32 v176, v176, v132\n"
  "v_mul_f32 v177, v177, v132\n"
  "ds_read_b128 v[16:19], v196 offset:0\n"
  "ds_read_b128 v[20:23], v196 offset:1024\n"
  "ds_read_b128 v[80:83], v196 offset:2048\n"
  "buffer_load_dwordx4 v242, s[40:43], s59 offen offset:2048 nt lds\n"
  "v_mul_f32 v178, v178, v132\n"
  "v_mul_f32 v179, v179, v132\n"
  "v_fma_f32 v176, v120, v133, v176\n"
  "v_fma_f32 v177, v121, v133, v177\n"
  "v_fma_f32 v178, v122, v133, v178\n"
  "v_fma_f32 v179, v123, v133, v179\n"
  "ds_read_b128 v[84:87], v196 offset:3072\n"
  "v_cvt_pk_bf16_f32 v176, v176, v177\n"
  "v_cvt_pk_bf16_f32 v177, v178, v179\n"
  "v_accvgpr_read_b32 v180, a28\n"
  "v_accvgpr_read_b32 v181, a29\n"
  "v_accvgpr_read_b32 v182, a30\n"
  "v_accvgpr_read_b32 v183, a31\n"
  "ds_write_b64 v143, v[176:177] offset:0\n"
  "v_mul_f32 v180, v180, v132\n"
  "v_mul_f32 v181, v181, v132\n"
  "v_mul_f32 v182, v182, v132\n"
  "v_mul_f32 v183, v183, v132\n"
  "v_fma_f32 v180, v124, v133, v180\n"
  "v_fma_f32 v181, v125, v133, v181\n"
  "buffer_load_dwordx4 v242, s[40:43], s59 offen offset:3072 nt lds\n"
  "v_fma_f32 v182, v126, v133, v182\n"
  "v_fma_f32 v183, v127, v133, v183\n"
  "v_cvt_pk_bf16_f32 v180, v180, v181\n"
  "v_cvt_pk_bf16_f32 v181, v182, v183\n"
  "s_add_u32 s59, s59, 4096\n"
  "s_add_u32 m0, s61, 36864\n"
  "ds_write_b64 v144, v[180:181] offset:0\n"
  "ds_read_b128 v[244:247], v145 offset:0\n"
  "ds_read_b128 v[248:251], v145 offset:1024\n"
  "s_waitcnt lgkmcnt(7)\n"
  "buffer_store_dwordx4 v[16:19], v188, s[52:55], 0 offen offset:128 nt\n"
  "s_waitcnt lgkmcnt(6)\n"
  "buffer_store_dwordx4 v[20:23], v188, s[52:55], s82 offen offset:128 nt\n"
  "s_waitcnt lgkmcnt(5)\n"
  "buffer_store_dwordx4 v[80:83], v188, s[52:55], s81 offen offset:128 nt\n"
  "s_waitcnt lgkmcnt(4)\n"
  "buffer_store_dwordx4 v[84:87], v188, s[52:55], s92 offen offset:128 nt\n"
  "s_waitcnt lgkmcnt(1)\n"
  "buffer_store_dwordx4 v[244:247], v189, s[52:55], 0 offen offset:128 nt\n"
  "s_waitcnt lgkmcnt(0)\n"
  "buffer_store_dwordx4 v[248:251], v189, s[52:55], s82 offen offset:128 nt\n"
  "buffer_load_dwordx4 v242, s[40:43], s59 offen offset:0 nt lds\n"
  "buffer_load_dwordx4 v242, s[40:43], s59 offen offset:1024 nt lds\n"
  "buffer_load_dwordx4 v242, s[40:43], s59 offen offset:2048 nt lds\n"
  "buffer_load_dwordx4 v242, s[40:43], s59 offen offset:3072 nt lds\n"
  "s_add_u32 s59, s59, 28672\n"
  "s_and_b32 s59, s59, 0x7ffff\n"
  "s_or_b32 s59, s59, s78\n"
  ".Lattn_FEND:\n"
  "s_add_u32 s52, s52, 319488\n"
  "s_addc_u32 s53, s53, 0\n"
  "s_mov_b32 s50, 0\n"
  "s_mov_b32 s98, 0\n"
  "s_mov_b32 s96, s90\n"
  "s_mov_b32 s97, s91\n"
  "s_mov_b32 s100, 0\n"
  "s_mov_b32 s101, 0\n"
  "s_waitcnt lgkmcnt(0)\n"
  "s_barrier\n"
  "s_branch .Lattn_BLOCK\n"
  ".Lattn_FINAL2:\n"
  "ds_read_b64_tr_b16 a[224:225], v216 offset:33024\n"
  "ds_read_b64_tr_b16 a[226:227], v216 offset:34048\n"
  "ds_read_b64_tr_b16 a[228:229], v224 offset:33024\n"
  "ds_read_b64_tr_b16 a[230:231], v224 offset:34048\n"
  "ds_read_b64_tr_b16 a[232:233], v217 offset:33024\n"
  "ds_read_b64_tr_b16 a[234:235], v217 offset:34048\n"
  "ds_read_b64_tr_b16 a[236:237], v225 offset:33024\n"
  "ds_read_b64_tr_b16 a[238:239], v225 offset:34048\n"
  "ds_read_b64_tr_b16 a[240:241], v218 offset:33024\n"
  "ds_read_b64_tr_b16 a[242:243], v218 offset:34048\n"
  "ds_read_b64_tr_b16 a[244:245], v226 offset:33024\n"
  "ds_read_b64_tr_b16 a[246:247], v226 offset:34048\n"
  "ds_read_b64_tr_b16 a[248:249], v219 offset:33024\n"
  "ds_read_b64_tr_b16 a[250:251], v219 offset:34048\n"
  "ds_read_b64_tr_b16 a[252:253], v227 offset:33024\n"
  "ds_read_b64_tr_b16 a[254:255], v227 offset:34048\n"
  "ds_read_b64_tr_b16 a[176:177], v220 offset:33024\n"
  "ds_read_b64_tr_b16 a[178:179], v220 offset:34048\n"
  "v_cvt_pk_bf16_f32 v164, v164, v165\n"
  "v_cvt_pk_bf16_f32 v165, v166, v167\n"
  "v_cvt_pk_bf16_f32 v166, v184, v185\n"
  "v_cvt_pk_bf16_f32 v167, v186, v187\n"
  "v_cvt_pk_bf16_f32 v162, v180, v181\n"
  "v_cvt_pk_bf16_f32 v163, v182, v183\n"
  "v_add_f32 v235, v235, v238\n"
  "v_add_f32 v236, v236, v239\n"
  "v_add_f32 v237, v237, v240\n"
  "v_mov_b32 v138, v235\n"
  "v_mov_b32 v139, v236\n"
  "v_mov_b32 v140, v237\n"
  "s_waitcnt lgkmcnt(0)\n"
  "v_mfma_f32_16x16x32_bf16 v[32:35], a[224:227], v[148:151], v[32:35]\n"
  "v_permlane32_swap_b32 v235, v138\n"
  "v_permlane32_swap_b32 v236, v139\n"
  "v_permlane32_swap_b32 v237, v140\n"
  "v_add_f32 v235, v235, v138\n"
  "v_mfma_f32_16x16x32_bf16 v[32:35], a[228:231], v[160:163], v[32:35]\n"
  "v_add_f32 v236, v236, v139\n"
  "v_add_f32 v237, v237, v140\n"
  "v_mov_b32 v138, v235\n"
  "v_mov_b32 v139, v236\n"
  "v_mfma_f32_16x16x32_bf16 v[96:99], a[228:231], v[164:167], v[96:99]\n"
  "ds_read_b64_tr_b16 a[180:181], v228 offset:33024\n"
  "v_mov_b32 v140, v237\n"
  "v_permlane16_swap_b32 v235, v138\n"
  "v_permlane16_swap_b32 v236, v139\n"
  "v_permlane16_swap_b32 v237, v140\n"
  "v_mfma_f32_16x16x32_bf16 v[96:99], a[224:227], v[152:155], v[96:99]\n"
  "ds_read_b64_tr_b16 a[182:183], v228 offset:34048\n"
  "v_add_f32 v235, v235, v138\n"
  "v_add_f32 v236, v236, v139\n"
  "v_add_f32 v237, v237, v140\n"
  "v_mov_b32 v130, v234\n"
  "v_mfma_f32_16x16x32_bf16 a[32:35], a[224:227], v[156:159], a[32:35]\n"
  "ds_read_b64_tr_b16 a[184:185], v221 offset:33024\n"
  "v_mov_b32 v131, v237\n"
  "v_xor_b32 v137, 0x2000, v191\n"
  "v_rcp_f32 v134, v235\n"
  "v_rcp_f32 v135, v236\n"
  "v_mfma_f32_16x16x32_bf16 a[36:39], a[232:235], v[156:159], a[36:39]\n"
  "ds_read_b64_tr_b16 a[186:187], v221 offset:34048\n"
  "ds_write_b64 v137, v[130:131] offset:7168\n"
  "v_mfma_f32_16x16x32_bf16 v[100:103], a[232:235], v[152:155], v[100:103]\n"
  "ds_read_b64_tr_b16 a[188:189], v229 offset:33024\n"
  "v_mfma_f32_16x16x32_bf16 v[100:103], a[236:239], v[164:167], v[100:103]\n"
  "ds_read_b64_tr_b16 a[190:191], v229 offset:34048\n"
  "v_mul_f32 v32, v32, v134\n"
  "v_mul_f32 v33, v33, v134\n"
  "v_mul_f32 v34, v34, v134\n"
  "v_mul_f32 v35, v35, v134\n"
  "v_mfma_f32_16x16x32_bf16 v[36:39], a[236:239], v[160:163], v[36:39]\n"
  "ds_read_b64_tr_b16 a[224:225], v222 offset:33024\n"
  "ds_write_b128 v191, a[32:35] offset:0\n"
  "v_cvt_pk_bf16_f32 v32, v32, v33\n"
  "v_cvt_pk_bf16_f32 v33, v34, v35\n"
  "v_mul_f32 v96, v96, v135\n"
  "v_mul_f32 v97, v97, v135\n"
  "v_mfma_f32_16x16x32_bf16 v[36:39], a[232:235], v[148:151], v[36:39]\n"
  "ds_read_b64_tr_b16 a[226:227], v222 offset:34048\n"
  "v_mul_f32 v98, v98, v135\n"
  "v_mul_f32 v99, v99, v135\n"
  "v_cvt_pk_bf16_f32 v96, v96, v97\n"
  "v_cvt_pk_bf16_f32 v97, v98, v99\n"
  "v_mfma_f32_16x16x32_bf16 v[40:43], a[240:243], v[148:151], v[40:43]\n"
  "s_waitcnt lgkmcnt(0)\n"
  "ds_write2st64_b64 v192, v[32:33], v[96:97] offset0:0 offset1:4\n"
  "v_mfma_f32_16x16x32_bf16 v[40:43], a[244:247], v[160:163], v[40:43]\n"
  "s_barrier\n"
  "ds_read_b64_tr_b16 a[228:229], v230 offset:33024\n"
  "v_mfma_f32_16x16x32_bf16 v[104:107], a[244:247], v[164:167], v[104:107]\n"
  "ds_read_b64_tr_b16 a[230:231], v230 offset:34048\n"
  "v_mul_f32 v36, v36, v134\n"
  "v_mul_f32 v37, v37, v134\n"
  "v_mul_f32 v38, v38, v134\n"
  "v_mul_f32 v39, v39, v134\n"
  "v_mfma_f32_16x16x32_bf16 v[104:107], a[240:243], v[152:155], v[104:107]\n"
  "ds_read_b64_tr_b16 a[232:233], v223 offset:33024\n"
  "ds_write_b128 v191, a[36:39] offset:1024\n"
  "v_cvt_pk_bf16_f32 v36, v36, v37\n"
  "v_cvt_pk_bf16_f32 v37, v38, v39\n"
  "v_mul_f32 v100, v100, v135\n"
  "v_mul_f32 v101, v101, v135\n"
  "v_mfma_f32_16x16x32_bf16 a[40:43], a[240:243], v[156:159], a[40:43]\n"
  "ds_read_b64_tr_b16 a[234:235], v223 offset:34048\n"
  "v_mul_f32 v102, v102, v135\n"
  "v_mul_f32 v103, v103, v135\n"
  "v_cvt_pk_bf16_f32 v100, v100, v101\n"
  "v_cvt_pk_bf16_f32 v101, v102, v103\n"
  "v_mfma_f32_16x16x32_bf16 a[44:47], a[248:251], v[156:159], a[44:47]\n"
  "ds_read_b64_tr_b16 a[236:237], v231 offset:33024\n"
  "ds_write2st64_b64 v193, v[36:37], v[100:101] offset0:0 offset1:4\n"
  "v_mfma_f32_16x16x32_bf16 v[108:111], a[248:251], v[152:155], v[108:111]\n"
  "ds_read_b64_tr_b16 a[238:239], v231 offset:34048\n"
  "ds_read_b64 v[128:129], v191 offset:7168\n"
  "v_mfma_f32_16x16x32_bf16 v[108:111], a[252:255], v[164:167], v[108:111]\n"
  "ds_read_b64_tr_b16 a[240:241], v216 offset:32768\n"
  "v_mul_f32 v40, v40, v134\n"
  "v_mul_f32 v41, v41, v134\n"
  "v_mul_f32 v42, v42, v134\n"
  "v_mul_f32 v43, v43, v134\n"
  "v_mfma_f32_16x16x32_bf16 v[44:47], a[252:255], v[160:163], v[44:47]\n"
  "ds_read_b64_tr_b16 a[242:243], v216 offset:33792\n"
  "ds_write_b128 v191, a[40:43] offset:2048\n"
  "v_cvt_pk_bf16_f32 v40, v40, v41\n"
  "v_cvt_pk_bf16_f32 v41, v42, v43\n"
  "v_mul_f32 v104, v104, v135\n"
  "v_mul_f32 v105, v105, v135\n"
  "v_mfma_f32_16x16x32_bf16 v[44:47], a[248:251], v[148:151], v[44:47]\n"
  "v_mul_f32 v106, v106, v135\n"
  "v_mul_f32 v107, v107, v135\n"
  "v_cvt_pk_bf16_f32 v104, v104, v105\n"
  "v_cvt_pk_bf16_f32 v105, v106, v107\n"
  "v_mfma_f32_16x16x32_bf16 v[48:51], a[176:179], v[148:151], v[48:51]\n"
  "ds_read_b64_tr_b16 a[244:245], v224 offset:32768\n"
  "ds_write2st64_b64 v194, v[40:41], v[104:105] offset0:0 offset1:4\n"
  "v_mfma_f32_16x16x32_bf16 v[48:51], a[180:183], v[160:163], v[48:51]\n"
  "ds_read_b64_tr_b16 a[246:247], v224 offset:33792\n"
  "v_mfma_f32_16x16x32_bf16 v[112:115], a[180:183], v[164:167], v[112:115]\n"
  "ds_read_b64_tr_b16 a[248:249], v217 offset:32768\n"
  "v_mul_f32 v44, v44, v134\n"
  "v_mul_f32 v45, v45, v134\n"
  "v_mul_f32 v46, v46, v134\n"
  "v_mul_f32 v47, v47, v134\n"
  "v_mfma_f32_16x16x32_bf16 v[112:115], a[176:179], v[152:155], v[112:115]\n"
  "ds_read_b64_tr_b16 a[250:251], v217 offset:33792\n"
  "ds_write_b128 v191, a[44:47] offset:3072\n"
  "v_cvt_pk_bf16_f32 v44, v44, v45\n"
  "v_cvt_pk_bf16_f32 v45, v46, v47\n"
  "v_mul_f32 v108, v108, v135\n"
  "v_mul_f32 v109, v109, v135\n"
  "v_mfma_f32_16x16x32_bf16 a[48:51], a[176:179], v[156:159], a[48:51]\n"
  "ds_read_b64_tr_b16 a[252:253], v225 offset:32768\n"
  "v_mul_f32 v110, v110, v135\n"
  "v_mul_f32 v111, v111, v135\n"
  "v_cvt_pk_bf16_f32 v108, v108, v109\n"
  "v_cvt_pk_bf16_f32 v109, v110, v111\n"
  "v_mfma_f32_16x16x32_bf16 a[52:55], a[184:187], v[156:159], a[52:55]\n"
  "ds_read_b64_tr_b16 a[254:255], v225 offset:33792\n"
  "ds_write2st64_b64 v195, v[44:45], v[108:109] offset0:0 offset1:4\n"
  "v_mfma_f32_16x16x32_bf16 v[116:119], a[184:187], v[152:155], v[116:119]\n"
  "ds_read_b64_tr_b16 a[176:177], v218 offset:32768\n"
  "ds_read_b128 v[32:35], v196 offset:0\n"
  "ds_read_b128 v[36:39], v196 offset:1024\n"
  "v_mfma_f32_16x16x32_bf16 v[116:119], a[188:191], v[164:167], v[116:119]\n"
  "ds_read_b128 v[96:99], v196 offset:2048\n"
  "ds_read_b128 v[100:103], v196 offset:3072\n"
  "v_mul_f32 v48, v48, v134\n"
  "v_mul_f32 v49, v49, v134\n"
  "v_mul_f32 v50, v50, v134\n"
  "v_mul_f32 v51, v51, v134\n"
  "v_mfma_f32_16x16x32_bf16 v[52:55], a[188:191], v[160:163], v[52:55]\n"
  "ds_write_b128 v191, a[48:51] offset:4096\n"
  "v_cvt_pk_bf16_f32 v48, v48, v49\n"
  "v_cvt_pk_bf16_f32 v49, v50, v51\n"
  "v_mul_f32 v112, v112, v135\n"
  "v_mul_f32 v113, v113, v135\n"
  "v_mfma_f32_16x16x32_bf16 v[52:55], a[184:187], v[148:151], v[52:55]\n"
  "ds_read_b64_tr_b16 a[178:179], v218 offset:33792\n"
  "v_mul_f32 v114, v114, v135\n"
  "v_mul_f32 v115, v115, v135\n"
  "v_cvt_pk_bf16_f32 v112, v112, v113\n"
  "v_cvt_pk_bf16_f32 v113, v114, v115\n"
  "v_mfma_f32_16x16x32_bf16 v[56:59], a[224:227], v[148:151], v[56:59]\n"
  "s_waitcnt lgkmcnt(13)\n"
  "ds_read_b64_tr_b16 a[180:181], v226 offset:32768\n"
  "ds_write2st64_b64 v192, v[48:49], v[112:113] offset0:0 offset1:4\n"
  "v_max_f32 v138, v130, v128\n"
  "v_sub_f32 v132, v130, v138\n"
  "v_sub_f32 v133, v128, v138\n"
  "v_exp_f32 v132, v132\n"
  "v_mfma_f32_16x16x32_bf16 v[56:59], a[228:231], v[160:163], v[56:59]\n"
  "ds_read_b64_tr_b16 a[182:183], v226 offset:33792\n"
  "s_waitcnt lgkmcnt(8)\n"
  "buffer_store_dwordx4 v[32:35], v188, s[52:55], 0 offen offset:256 nt\n"
  "v_exp_f32 v133, v133\n"
  "v_add_u32 v141, s94, v192\n"
  "v_add_u32 v142, s94, v193\n"
  "v_mul_f32 v136, v131, v132\n"
  "v_mfma_f32_16x16x32_bf16 v[120:123], a[228:231], v[164:167], v[120:123]\n"
  "ds_read_b64_tr_b16 a[184:185], v219 offset:32768\n"
  "v_fma_f32 v136, v129, v133, v136\n"
  "v_rcp_f32 v136, v136\n"
  "v_add_u32 v143, s94, v194\n"
  "v_add_u32 v144, s94, v195\n"
  "v_mfma_f32_16x16x32_bf16 v[120:123], a[224:227], v[152:155], v[120:123]\n"
  "ds_read_b64_tr_b16 a[186:187], v219 offset:33792\n"
  "ds_write_b128 v191, a[52:55] offset:5120\n"
  "v_add_u32 v145, s94, v196\n"
  "v_mul_f32 v132, v132, v136\n"
  "v_mul_f32 v133, v133, v136\n"
  "v_mul_f32 v52, v52, v134\n"
  "v_mfma_f32_16x16x32_bf16 a[56:59], a[224:227], v[156:159], a[56:59]\n"
  "ds_read_b64_tr_b16 a[188:189], v227 offset:32768\n"
  "s_waitcnt lgkmcnt(11)\n"
  "buffer_store_dwordx4 v[36:39], v188, s[52:55], s82 offen offset:256 nt\n"
  "v_mul_f32 v53, v53, v134\n"
  "v_mul_f32 v54, v54, v134\n"
  "v_mul_f32 v55, v55, v134\n"
  "v_cvt_pk_bf16_f32 v52, v52, v53\n"
  "v_mfma_f32_16x16x32_bf16 a[60:63], a[232:235], v[156:159], a[60:63]\n"
  "ds_read_b64_tr_b16 a[190:191], v227 offset:33792\n"
  "v_cvt_pk_bf16_f32 v53, v54, v55\n"
  "v_mul_f32 v116, v116, v135\n"
  "v_mul_f32 v117, v117, v135\n"
  "v_mul_f32 v118, v118, v135\n"
  "v_mfma_f32_16x16x32_bf16 v[124:127], a[232:235], v[152:155], v[124:127]\n"
  "ds_read_b64_tr_b16 a[224:225], v220 offset:32768\n"
  "s_waitcnt lgkmcnt(12)\n"
  "buffer_store_dwordx4 v[96:99], v188, s[52:55], s81 offen offset:256 nt\n"
  "v_mul_f32 v119, v119, v135\n"
  "v_cvt_pk_bf16_f32 v116, v116, v117\n"
  "v_cvt_pk_bf16_f32 v117, v118, v119\n"
  "v_mfma_f32_16x16x32_bf16 v[124:127], a[236:239], v[164:167], v[124:127]\n"
  "ds_write2st64_b64 v193, v[52:53], v[116:117] offset0:0 offset1:4\n"
  "v_mul_f32 v56, v56, v134\n"
  "v_mul_f32 v57, v57, v134\n"
  "v_mul_f32 v58, v58, v134\n"
  "v_mul_f32 v59, v59, v134\n"
  "v_mfma_f32_16x16x32_bf16 v[60:63], a[236:239], v[160:163], v[60:63]\n"
  "ds_read_b64_tr_b16 a[226:227], v220 offset:33792\n"
  "ds_read_b64_tr_b16 a[228:229], v228 offset:32768\n"
  "ds_write_b128 v191, a[56:59] offset:6144\n"
  "v_cvt_pk_bf16_f32 v56, v56, v57\n"
  "v_cvt_pk_bf16_f32 v57, v58, v59\n"
  "v_mul_f32 v120, v120, v135\n"
  "v_mul_f32 v121, v121, v135\n"
  "v_mfma_f32_16x16x32_bf16 v[60:63], a[232:235], v[148:151], v[60:63]\n"
  "ds_read_b64_tr_b16 a[230:231], v228 offset:33792\n"
  "s_waitcnt lgkmcnt(15)\n"
  "buffer_store_dwordx4 v[100:103], v188, s[52:55], s92 offen offset:256 nt\n"
  "v_mul_f32 v122, v122, v135\n"
  "v_mul_f32 v123, v123, v135\n"
  "v_cvt_pk_bf16_f32 v120, v120, v121\n"
  "v_cvt_pk_bf16_f32 v121, v122, v123\n"
  "v_mfma_f32_16x16x32_bf16 v[0:3], a[240:243], v[148:151], v[0:3]\n"
  "ds_write2st64_b64 v194, v[56:57], v[120:121] offset0:0 offset1:4\n"
  "v_mfma_f32_16x16x32_bf16 v[0:3], a[244:247], v[160:163], v[0:3]\n"
  "ds_read_b64_tr_b16 a[232:233], v221 offset:32768\n"
  "v_mfma_f32_16x16x32_bf16 v[64:67], a[244:247], v[164:167], v[64:67]\n"
  "ds_read_b64_tr_b16 a[234:235], v221 offset:33792\n"
  "ds_read_b64_tr_b16 a[236:237], v229 offset:32768\n"
  "v_mul_f32 v60, v60, v134\n"
  "v_mul_f32 v61, v61, v134\n"
  "v_mul_f32 v62, v62, v134\n"
  "v_mul_f32 v63, v63, v134\n"
  "v_mfma_f32_16x16x32_bf16 v[64:67], a[240:243], v[152:155], v[64:67]\n"
  "ds_write_b128 v191, a[60:63] offset:7168\n"
  "v_cvt_pk_bf16_f32 v60, v60, v61\n"
  "v_cvt_pk_bf16_f32 v61, v62, v63\n"
  "v_mul_f32 v124, v124, v135\n"
  "v_mul_f32 v125, v125, v135\n"
  "v_mfma_f32_16x16x32_bf16 a[0:3], a[240:243], v[156:159], a[0:3]\n"
  "s_waitcnt lgkmcnt(11)\n"
  "ds_read_b64_tr_b16 a[238:239], v229 offset:33792\n"
  "v_mul_f32 v126, v126, v135\n"
  "v_mul_f32 v127, v127, v135\n"
  "v_cvt_pk_bf16_f32 v124, v124, v125\n"
  "v_cvt_pk_bf16_f32 v125, v126, v127\n"
  "v_mfma_f32_16x16x32_bf16 a[4:7], a[248:251], v[156:159], a[4:7]\n"
  "ds_read_b64_tr_b16 a[240:241], v222 offset:32768\n"
  "ds_write2st64_b64 v195, v[60:61], v[124:125] offset0:0 offset1:4\n"
  "v_mfma_f32_16x16x32_bf16 v[68:71], a[248:251], v[152:155], v[68:71]\n"
  "ds_read_b128 v[48:51], v196 offset:0\n"
  "ds_read_b128 v[52:55], v196 offset:1024\n"
  "ds_read_b128 v[112:115], v196 offset:2048\n"
  "v_mfma_f32_16x16x32_bf16 v[68:71], a[252:255], v[164:167], v[68:71]\n"
  "ds_read_b64_tr_b16 a[242:243], v222 offset:33792\n"
  "ds_read_b64_tr_b16 a[244:245], v230 offset:32768\n"
  "ds_read_b128 v[116:119], v196 offset:3072\n"
  "v_mul_f32 v0, v0, v134\n"
  "v_mul_f32 v1, v1, v134\n"
  "v_mul_f32 v2, v2, v134\n"
  "v_mul_f32 v3, v3, v134\n"
  "v_mfma_f32_16x16x32_bf16 v[4:7], a[252:255], v[160:163], v[4:7]\n"
  "v_cvt_pk_bf16_f32 v0, v0, v1\n"
  "v_cvt_pk_bf16_f32 v1, v2, v3\n"
  "v_mul_f32 v64, v64, v135\n"
  "v_mul_f32 v65, v65, v135\n"
  "v_mfma_f32_16x16x32_bf16 v[4:7], a[248:251], v[148:151], v[4:7]\n"
  "s_waitcnt lgkmcnt(0)\n"
  "ds_read_b64_tr_b16 a[246:247], v230 offset:33792\n"
  "v_mul_f32 v66, v66, v135\n"
  "v_mul_f32 v67, v67, v135\n"
  "v_cvt_pk_bf16_f32 v64, v64, v65\n"
  "v_cvt_pk_bf16_f32 v65, v66, v67\n"
  "v_mfma_f32_16x16x32_bf16 v[8:11], a[176:179], v[148:151], v[8:11]\n"
  "s_barrier\n"
  "ds_read_b64_tr_b16 a[248:249], v223 offset:32768\n"
  "ds_read_b64_tr_b16 a[250:251], v223 offset:33792\n"
  "ds_write2st64_b64 v192, v[0:1], v[64:65] offset0:0 offset1:4\n"
  "v_mfma_f32_16x16x32_bf16 v[8:11], a[180:183], v[160:163], v[8:11]\n"
  "ds_read_b128 v[40:43], v137 offset:0\n"
  "ds_read_b128 v[44:47], v137 offset:1024\n"
  "ds_read_b128 v[104:107], v137 offset:2048\n"
  "buffer_store_dwordx4 v[48:51], v188, s[52:55], 0 offen offset:384 nt\n"
  "v_mfma_f32_16x16x32_bf16 v[72:75], a[180:183], v[164:167], v[72:75]\n"
  "ds_read_b64_tr_b16 a[252:253], v231 offset:32768\n"
  "ds_read_b128 v[108:111], v137 offset:3072\n"
  "ds_read_b128 v[56:59], v137 offset:4096\n"
  "v_mul_f32 v4, v4, v134\n"
  "v_mul_f32 v5, v5, v134\n"
  "v_mul_f32 v6, v6, v134\n"
  "v_mul_f32 v7, v7, v134\n"
  "v_cvt_pk_bf16_f32 v4, v4, v5\n"
  "v_cvt_pk_bf16_f32 v5, v6, v7\n"
  "v_mfma_f32_16x16x32_bf16 v[72:75], a[176:179], v[152:155], v[72:75]\n"
  "ds_read_b128 v[60:63], v137 offset:5120\n"
  "ds_read_b128 v[120:123], v137 offset:6144\n"
  "ds_read_b128 v[124:127], v137 offset:7168\n"
  "buffer_store_dwordx4 v[52:55], v188, s[52:55], s82 offen offset:384 nt\n"
  "v_mul_f32 v68, v68, v135\n"
  "v_mul_f32 v69, v69, v135\n"
  "v_mul_f32 v70, v70, v135\n"
  "v_mul_f32 v71, v71, v135\n"
  "v_cvt_pk_bf16_f32 v68, v68, v69\n"
  "v_cvt_pk_bf16_f32 v69, v70, v71\n"
  "v_mfma_f32_16x16x32_bf16 a[8:11], a[176:179], v[156:159], a[8:11]\n"
  "ds_read_b64_tr_b16 a[254:255], v231 offset:33792\n"
  "ds_write2st64_b64 v193, v[4:5], v[68:69] offset0:0 offset1:4\n"
  "v_accvgpr_read_b32 v168, a0\n"
  "v_accvgpr_read_b32 v169, a1\n"
  "v_accvgpr_read_b32 v170, a2\n"
  "v_accvgpr_read_b32 v171, a3\n"
  "v_mul_f32 v168, v168, v132\n"
  "v_mul_f32 v169, v169, v132\n"
  "v_mfma_f32_16x16x32_bf16 a[12:15], a[184:187], v[156:159], a[12:15]\n"
  "buffer_store_dwordx4 v[112:115], v188, s[52:55], s81 offen offset:384 nt\n"
  "v_mul_f32 v170, v170, v132\n"
  "v_mul_f32 v171, v171, v132\n"
  "s_waitcnt lgkmcnt(10)\n"
  "v_fma_f32 v168, v40, v133, v168\n"
  "v_fma_f32 v169, v41, v133, v169\n"
  "v_fma_f32 v170, v42, v133, v170\n"
  "v_fma_f32 v171, v43, v133, v171\n"
  "v_mfma_f32_16x16x32_bf16 v[76:79], a[184:187], v[152:155], v[76:79]\n"
  "v_cvt_pk_bf16_f32 v168, v168, v169\n"
  "v_cvt_pk_bf16_f32 v169, v170, v171\n"
  "v_mfma_f32_16x16x32_bf16 v[76:79], a[188:191], v[164:167], v[76:79]\n"
  "ds_write_b64 v141, v[168:169] offset:0\n"
  "v_mul_f32 v8, v8, v134\n"
  "v_mul_f32 v9, v9, v134\n"
  "v_mul_f32 v10, v10, v134\n"
  "v_mul_f32 v11, v11, v134\n"
  "v_cvt_pk_bf16_f32 v8, v8, v9\n"
  "v_cvt_pk_bf16_f32 v9, v10, v11\n"
  "v_mfma_f32_16x16x32_bf16 v[12:15], a[188:191], v[160:163], v[12:15]\n"
  "buffer_store_dwordx4 v[116:119], v188, s[52:55], s92 offen offset:384 nt\n"
  "v_mul_f32 v72, v72, v135\n"
  "v_mul_f32 v73, v73, v135\n"
  "v_mul_f32 v74, v74, v135\n"
  "v_mul_f32 v75, v75, v135\n"
  "v_cvt_pk_bf16_f32 v72, v72, v73\n"
  "v_cvt_pk_bf16_f32 v73, v74, v75\n"
  "v_mfma_f32_16x16x32_bf16 v[12:15], a[184:187], v[148:151], v[12:15]\n"
  "ds_write2st64_b64 v194, v[8:9], v[72:73] offset0:0 offset1:4\n"
  "v_accvgpr_read_b32 v172, a4\n"
  "v_accvgpr_read_b32 v173, a5\n"
  "v_accvgpr_read_b32 v174, a6\n"
  "v_accvgpr_read_b32 v175, a7\n"
  "v_mul_f32 v172, v172, v132\n"
  "v_mul_f32 v173, v173, v132\n"
  "v_mfma_f32_16x16x32_bf16 v[16:19], a[224:227], v[148:151], v[16:19]\n"
  "v_mul_f32 v174, v174, v132\n"
  "v_mul_f32 v175, v175, v132\n"
  "s_waitcnt lgkmcnt(11)\n"
  "v_fma_f32 v172, v44, v133, v172\n"
  "v_fma_f32 v173, v45, v133, v173\n"
  "v_fma_f32 v174, v46, v133, v174\n"
  "v_fma_f32 v175, v47, v133, v175\n"
  "v_mfma_f32_16x16x32_bf16 v[16:19], a[228:231], v[160:163], v[16:19]\n"
  "v_cvt_pk_bf16_f32 v172, v172, v173\n"
  "v_cvt_pk_bf16_f32 v173, v174, v175\n"
  "v_mfma_f32_16x16x32_bf16 v[80:83], a[228:231], v[164:167], v[80:83]\n"
  "ds_write_b64 v142, v[172:173] offset:0\n"
  "v_mul_f32 v12, v12, v134\n"
  "v_mul_f32 v13, v13, v134\n"
  "v_mul_f32 v14, v14, v134\n"
  "v_mul_f32 v15, v15, v134\n"
  "v_cvt_pk_bf16_f32 v12, v12, v13\n"
  "v_cvt_pk_bf16_f32 v13, v14, v15\n"
  "v_mfma_f32_16x16x32_bf16 v[80:83], a[224:227], v[152:155], v[80:83]\n"
  "v_mul_f32 v76, v76, v135\n"
  "v_mul_f32 v77, v77, v135\n"
  "v_mul_f32 v78, v78, v135\n"
  "v_mul_f32 v79, v79, v135\n"
  "v_cvt_pk_bf16_f32 v76, v76, v77\n"
  "v_cvt_pk_bf16_f32 v77, v78, v79\n"
  "v_mfma_f32_16x16x32_bf16 a[16:19], a[224:227], v[156:159], a[16:19]\n"
  "ds_write2st64_b64 v195, v[12:13], v[76:77] offset0:0 offset1:4\n"
  "v_accvgpr_read_b32 v176, a8\n"
  "v_accvgpr_read_b32 v177, a9\n"
  "v_accvgpr_read_b32 v178, a10\n"
  "v_accvgpr_read_b32 v179, a11\n"
  "v_mul_f32 v176, v176, v132\n"
  "v_mul_f32 v177, v177, v132\n"
  "v_mfma_f32_16x16x32_bf16 a[20:23], a[232:235], v[156:159], a[20:23]\n"
  "ds_read_b128 v[0:3], v196 offset:0\n"
  "ds_read_b128 v[4:7], v196 offset:1024\n"
  "ds_read_b128 v[64:67], v196 offset:2048\n"
  "v_mul_f32 v178, v178, v132\n"
  "v_mul_f32 v179, v179, v132\n"
  "s_waitcnt lgkmcnt(15)\n"
  "v_fma_f32 v176, v104, v133, v176\n"
  "v_fma_f32 v177, v105, v133, v177\n"
  "v_fma_f32 v178, v106, v133, v178\n"
  "v_fma_f32 v179, v107, v133, v179\n"
  "v_mfma_f32_16x16x32_bf16 v[84:87], a[232:235], v[152:155], v[84:87]\n"
  "ds_read_b128 v[68:71], v196 offset:3072\n"
  "v_cvt_pk_bf16_f32 v176, v176, v177\n"
  "v_cvt_pk_bf16_f32 v177, v178, v179\n"
  "v_mfma_f32_16x16x32_bf16 v[84:87], a[236:239], v[164:167], v[84:87]\n"
  "ds_write_b64 v143, v[176:177] offset:0\n"
  "v_mul_f32 v16, v16, v134\n"
  "v_mul_f32 v17, v17, v134\n"
  "v_mul_f32 v18, v18, v134\n"
  "v_mul_f32 v19, v19, v134\n"
  "v_cvt_pk_bf16_f32 v16, v16, v17\n"
  "v_cvt_pk_bf16_f32 v17, v18, v19\n"
  "v_mfma_f32_16x16x32_bf16 v[20:23], a[236:239], v[160:163], v[20:23]\n"
  "v_mul_f32 v80, v80, v135\n"
  "v_mul_f32 v81, v81, v135\n"
  "v_mul_f32 v82, v82, v135\n"
  "v_mul_f32 v83, v83, v135\n"
  "v_cvt_pk_bf16_f32 v80, v80, v81\n"
  "v_cvt_pk_bf16_f32 v81, v82, v83\n"
  "v_mfma_f32_16x16x32_bf16 v[20:23], a[232:235], v[148:151], v[20:23]\n"
  "ds_write2st64_b64 v192, v[16:17], v[80:81] offset0:0 offset1:4\n"
  "v_accvgpr_read_b32 v180, a12\n"
  "v_accvgpr_read_b32 v181, a13\n"
  "v_accvgpr_read_b32 v182, a14\n"
  "v_accvgpr_read_b32 v183, a15\n"
  "v_mul_f32 v180, v180, v132\n"
  "v_mul_f32 v181, v181, v132\n"
  "v_mfma_f32_16x16x32_bf16 v[24:27], a[240:243], v[148:151], v[24:27]\n"
  "s_waitcnt lgkmcnt(8)\n"
  "s_waitcnt lgkmcnt(5)\n"
  "buffer_store_dwordx4 v[0:3], v188, s[52:55], 0 offen offset:0 nt\n"
  "v_mul_f32 v182, v182, v132\n"
  "v_mul_f32 v183, v183, v132\n"
  "v_fma_f32 v180, v108, v133, v180\n"
  "v_fma_f32 v181, v109, v133, v181\n"
  "v_fma_f32 v182, v110, v133, v182\n"
  "v_fma_f32 v183, v111, v133, v183\n"
  "v_mfma_f32_16x16x32_bf16 v[24:27], a[244:247], v[160:163], v[24:27]\n"
  "v_cvt_pk_bf16_f32 v180, v180, v181\n"
  "v_cvt_pk_bf16_f32 v181, v182, v183\n"
  "v_mfma_f32_16x16x32_bf16 v[88:91], a[244:247], v[164:167], v[88:91]\n"
  "ds_write_b64 v144, v[180:181] offset:0\n"
  "v_mul_f32 v20, v20, v134\n"
  "v_mul_f32 v21, v21, v134\n"
  "v_mul_f32 v22, v22, v134\n"
  "v_mul_f32 v23, v23, v134\n"
  "v_cvt_pk_bf16_f32 v20, v20, v21\n"
  "v_cvt_pk_bf16_f32 v21, v22, v23\n"
  "v_mfma_f32_16x16x32_bf16 v[88:91], a[240:243], v[152:155], v[88:91]\n"
  "ds_read_b128 v[244:247], v145 offset:0\n"
  "ds_read_b128 v[248:251], v145 offset:1024\n"
  "s_waitcnt lgkmcnt(7)\n"
  "buffer_store_dwordx4 v[4:7], v188, s[52:55], s82 offen offset:0 nt\n"
  "v_mul_f32 v84, v84, v135\n"
  "v_mul_f32 v85, v85, v135\n"
  "v_mul_f32 v86, v86, v135\n"
  "v_mul_f32 v87, v87, v135\n"
  "v_cvt_pk_bf16_f32 v84, v84, v85\n"
  "v_cvt_pk_bf16_f32 v85, v86, v87\n"
  "v_mfma_f32_16x16x32_bf16 a[24:27], a[240:243], v[156:159], a[24:27]\n"
  "ds_write2st64_b64 v193, v[20:21], v[84:85] offset0:0 offset1:4\n"
  "v_accvgpr_read_b32 v168, a16\n"
  "v_accvgpr_read_b32 v169, a17\n"
  "v_accvgpr_read_b32 v170, a18\n"
  "v_accvgpr_read_b32 v171, a19\n"
  "v_mul_f32 v168, v168, v132\n"
  "v_mul_f32 v169, v169, v132\n"
  "v_mfma_f32_16x16x32_bf16 a[28:31], a[248:251], v[156:159], a[28:31]\n"
  "s_waitcnt lgkmcnt(7)\n"
  "buffer_store_dwordx4 v[64:67], v188, s[52:55], s81 offen offset:0 nt\n"
  "v_mul_f32 v170, v170, v132\n"
  "v_mul_f32 v171, v171, v132\n"
  "v_fma_f32 v168, v56, v133, v168\n"
  "v_fma_f32 v169, v57, v133, v169\n"
  "v_fma_f32 v170, v58, v133, v170\n"
  "v_fma_f32 v171, v59, v133, v171\n"
  "v_mfma_f32_16x16x32_bf16 v[92:95], a[248:251], v[152:155], v[92:95]\n"
  "v_cvt_pk_bf16_f32 v168, v168, v169\n"
  "v_cvt_pk_bf16_f32 v169, v170, v171\n"
  "v_mfma_f32_16x16x32_bf16 v[92:95], a[252:255], v[164:167], v[92:95]\n"
  "ds_write_b64 v141, v[168:169] offset:0\n"
  "v_mul_f32 v24, v24, v134\n"
  "v_mul_f32 v25, v25, v134\n"
  "v_mul_f32 v26, v26, v134\n"
  "v_mul_f32 v27, v27, v134\n"
  "v_cvt_pk_bf16_f32 v24, v24, v25\n"
  "v_cvt_pk_bf16_f32 v25, v26, v27\n"
  "v_mfma_f32_16x16x32_bf16 v[28:31], a[252:255], v[160:163], v[28:31]\n"
  "s_waitcnt lgkmcnt(7)\n"
  "buffer_store_dwordx4 v[68:71], v188, s[52:55], s92 offen offset:0 nt\n"
  "v_mul_f32 v88, v88, v135\n"
  "v_mul_f32 v89, v89, v135\n"
  "v_mul_f32 v90, v90, v135\n"
  "v_mul_f32 v91, v91, v135\n"
  "v_cvt_pk_bf16_f32 v88, v88, v89\n"
  "v_cvt_pk_bf16_f32 v89, v90, v91\n"
  "v_mfma_f32_16x16x32_bf16 v[28:31], a[248:251], v[148:151], v[28:31]\n"
  "ds_write2st64_b64 v194, v[24:25], v[88:89] offset0:0 offset1:4\n"
  "v_accvgpr_read_b32 v172, a20\n"
  "v_accvgpr_read_b32 v173, a21\n"
  "v_accvgpr_read_b32 v174, a22\n"
  "v_accvgpr_read_b32 v175, a23\n"
  "v_mul_f32 v172, v172, v132\n"
  "v_mul_f32 v173, v173, v132\n"
  "s_waitcnt lgkmcnt(4)\n"
  "buffer_store_dwordx4 v[244:247], v189, s[52:55], 0 offen offset:0 nt\n"
  "v_mul_f32 v174, v174, v132\n"
  "v_mul_f32 v175, v175, v132\n"
  "v_fma_f32 v172, v60, v133, v172\n"
  "v_fma_f32 v173, v61, v133, v173\n"
  "v_fma_f32 v174, v62, v133, v174\n"
  "v_fma_f32 v175, v63, v133, v175\n"
  "v_cvt_pk_bf16_f32 v172, v172, v173\n"
  "v_cvt_pk_bf16_f32 v173, v174, v175\n"
  "ds_write_b64 v142, v[172:173] offset:0\n"
  "v_mul_f32 v28, v28, v134\n"
  "v_mul_f32 v29, v29, v134\n"
  "v_mul_f32 v30, v30, v134\n"
  "v_mul_f32 v31, v31, v134\n"
  "v_cvt_pk_bf16_f32 v28, v28, v29\n"
  "v_cvt_pk_bf16_f32 v29, v30, v31\n"
  "s_waitcnt lgkmcnt(4)\n"
  "buffer_store_dwordx4 v[248:251], v189, s[52:55], s82 offen offset:0 nt\n"
  "v_mul_f32 v92, v92, v135\n"
  "v_mul_f32 v93, v93, v135\n"
  "v_mul_f32 v94, v94, v135\n"
  "v_mul_f32 v95, v95, v135\n"
  "v_cvt_pk_bf16_f32 v92, v92, v93\n"
  "v_cvt_pk_bf16_f32 v93, v94, v95\n"
  "ds_write2st64_b64 v195, v[28:29], v[92:93] offset0:0 offset1:4\n"
  "v_accvgpr_read_b32 v176, a24\n"
  "v_accvgpr_read_b32 v177, a25\n"
  "v_accvgpr_read_b32 v178, a26\n"
  "v_accvgpr_read_b32 v179, a27\n"
  "v_mul_f32 v176, v176, v132\n"
  "v_mul_f32 v177, v177, v132\n"
  "ds_read_b128 v[16:19], v196 offset:0\n"
  "ds_read_b128 v[20:23], v196 offset:1024\n"
  "ds_read_b128 v[80:83], v196 offset:2048\n"
  "v_mul_f32 v178, v178, v132\n"
  "v_mul_f32 v179, v179, v132\n"
  "v_fma_f32 v176, v120, v133, v176\n"
  "v_fma_f32 v177, v121, v133, v177\n"
  "v_fma_f32 v178, v122, v133, v178\n"
  "v_fma_f32 v179, v123, v133, v179\n"
  "ds_read_b128 v[84:87], v196 offset:3072\n"
  "v_cvt_pk_bf16_f32 v176, v176, v177\n"
  "v_cvt_pk_bf16_f32 v177, v178, v179\n"
  "v_accvgpr_read_b32 v180, a28\n"
  "v_accvgpr_read_b32 v181, a29\n"
  "v_accvgpr_read_b32 v182, a30\n"
  "v_accvgpr_read_b32 v183, a31\n"
  "ds_write_b64 v143, v[176:177] offset:0\n"
  "v_mul_f32 v180, v180, v132\n"
  "v_mul_f32 v181, v181, v132\n"
  "v_mul_f32 v182, v182, v132\n"
  "v_mul_f32 v183, v183, v132\n"
  "v_fma_f32 v180, v124, v133, v180\n"
  "v_fma_f32 v181, v125, v133, v181\n"
  "v_fma_f32 v182, v126, v133, v182\n"
  "v_fma_f32 v183, v127, v133, v183\n"
  "v_cvt_pk_bf16_f32 v180, v180, v181\n"
  "v_cvt_pk_bf16_f32 v181, v182, v183\n"
  "ds_write_b64 v144, v[180:181] offset:0\n"
  "ds_read_b128 v[244:247], v145 offset:0\n"
  "ds_read_b128 v[248:251], v145 offset:1024\n"
  "s_waitcnt lgkmcnt(7)\n"
  "buffer_store_dwordx4 v[16:19], v188, s[52:55], 0 offen offset:128 nt\n"
  "s_waitcnt lgkmcnt(6)\n"
  "buffer_store_dwordx4 v[20:23], v188, s[52:55], s82 offen offset:128 nt\n"
  "s_waitcnt lgkmcnt(5)\n"
  "buffer_store_dwordx4 v[80:83], v188, s[52:55], s81 offen offset:128 nt\n"
  "s_waitcnt lgkmcnt(4)\n"
  "buffer_store_dwordx4 v[84:87], v188, s[52:55], s92 offen offset:128 nt\n"
  "s_waitcnt lgkmcnt(1)\n"
  "buffer_store_dwordx4 v[244:247], v189, s[52:55], 0 offen offset:128 nt\n"
  "s_waitcnt lgkmcnt(0)\n"
  "buffer_store_dwordx4 v[248:251], v189, s[52:55], s82 offen offset:128 nt\n"
  "s_endpgm\n"
  ".Lattn_FINAL3:\n"
  "ds_read_b64_tr_b16 a[224:225], v216 offset:32768\n"
  "ds_read_b64_tr_b16 a[226:227], v216 offset:33792\n"
  "ds_read_b64_tr_b16 a[228:229], v224 offset:32768\n"
  "ds_read_b64_tr_b16 a[230:231], v224 offset:33792\n"
  "ds_read_b64_tr_b16 a[232:233], v217 offset:32768\n"
  "ds_read_b64_tr_b16 a[234:235], v217 offset:33792\n"
  "ds_read_b64_tr_b16 a[236:237], v225 offset:32768\n"
  "ds_read_b64_tr_b16 a[238:239], v225 offset:33792\n"
  "ds_read_b64_tr_b16 a[240:241], v218 offset:32768\n"
  "ds_read_b64_tr_b16 a[242:243], v218 offset:33792\n"
  "ds_read_b64_tr_b16 a[244:245], v226 offset:32768\n"
  "ds_read_b64_tr_b16 a[246:247], v226 offset:33792\n"
  "ds_read_b64_tr_b16 a[248:249], v219 offset:32768\n"
  "ds_read_b64_tr_b16 a[250:251], v219 offset:33792\n"
  "ds_read_b64_tr_b16 a[252:253], v227 offset:32768\n"
  "ds_read_b64_tr_b16 a[254:255], v227 offset:33792\n"
  "ds_read_b64_tr_b16 a[176:177], v220 offset:32768\n"
  "ds_read_b64_tr_b16 a[178:179], v220 offset:33792\n"
  "v_cvt_pk_bf16_f32 v164, v164, v165\n"
  "v_cvt_pk_bf16_f32 v165, v166, v167\n"
  "v_cvt_pk_bf16_f32 v166, v184, v185\n"
  "v_cvt_pk_bf16_f32 v167, v186, v187\n"
  "v_cvt_pk_bf16_f32 v162, v180, v181\n"
  "v_cvt_pk_bf16_f32 v163, v182, v183\n"
  "v_add_f32 v235, v235, v238\n"
  "v_add_f32 v236, v236, v239\n"
  "v_add_f32 v237, v237, v240\n"
  "v_mov_b32 v138, v235\n"
  "v_mov_b32 v139, v236\n"
  "v_mov_b32 v140, v237\n"
  "s_waitcnt lgkmcnt(0)\n"
  "v_mfma_f32_16x16x32_bf16 v[0:3], a[224:227], v[148:151], v[0:3]\n"
  "v_permlane32_swap_b32 v235, v138\n"
  "v_permlane32_swap_b32 v236, v139\n"
  "v_permlane32_swap_b32 v237, v140\n"
  "v_add_f32 v235, v235, v138\n"
  "v_mfma_f32_16x16x32_bf16 v[0:3], a[228:231], v[160:163], v[0:3]\n"
  "v_add_f32 v236, v236, v139\n"
  "v_add_f32 v237, v237, v140\n"
  "v_mov_b32 v138, v235\n"
  "v_mov_b32 v139, v236\n"
  "v_mfma_f32_16x16x32_bf16 v[64:67], a[228:231], v[164:167], v[64:67]\n"
  "ds_read_b64_tr_b16 a[180:181], v228 offset:32768\n"
  "v_mov_b32 v140, v237\n"
  "v_permlane16_swap_b32 v235, v138\n"
  "v_permlane16_swap_b32 v236, v139\n"
  "v_permlane16_swap_b32 v237, v140\n"
  "v_mfma_f32_16x16x32_bf16 v[64:67], a[224:227], v[152:155], v[64:67]\n"
  "ds_read_b64_tr_b16 a[182:183], v228 offset:33792\n"
  "v_add_f32 v235, v235, v138\n"
  "v_add_f32 v236, v236, v139\n"
  "v_add_f32 v237, v237, v140\n"
  "v_mov_b32 v130, v234\n"
  "v_mfma_f32_16x16x32_bf16 a[0:3], a[224:227], v[156:159], a[0:3]\n"
  "ds_read_b64_tr_b16 a[184:185], v221 offset:32768\n"
  "v_mov_b32 v131, v237\n"
  "v_xor_b32 v137, 0x2000, v191\n"
  "v_rcp_f32 v134, v235\n"
  "v_rcp_f32 v135, v236\n"
  "v_mfma_f32_16x16x32_bf16 a[4:7], a[232:235], v[156:159], a[4:7]\n"
  "ds_read_b64_tr_b16 a[186:187], v221 offset:33792\n"
  "ds_write_b64 v137, v[130:131] offset:7168\n"
  "v_mfma_f32_16x16x32_bf16 v[68:71], a[232:235], v[152:155], v[68:71]\n"
  "ds_read_b64_tr_b16 a[188:189], v229 offset:32768\n"
  "v_mfma_f32_16x16x32_bf16 v[68:71], a[236:239], v[164:167], v[68:71]\n"
  "ds_read_b64_tr_b16 a[190:191], v229 offset:33792\n"
  "v_mul_f32 v0, v0, v134\n"
  "v_mul_f32 v1, v1, v134\n"
  "v_mul_f32 v2, v2, v134\n"
  "v_mul_f32 v3, v3, v134\n"
  "v_mfma_f32_16x16x32_bf16 v[4:7], a[236:239], v[160:163], v[4:7]\n"
  "ds_read_b64_tr_b16 a[224:225], v222 offset:32768\n"
  "ds_write_b128 v191, a[0:3] offset:0\n"
  "v_cvt_pk_bf16_f32 v0, v0, v1\n"
  "v_cvt_pk_bf16_f32 v1, v2, v3\n"
  "v_mul_f32 v64, v64, v135\n"
  "v_mul_f32 v65, v65, v135\n"
  "v_mfma_f32_16x16x32_bf16 v[4:7], a[232:235], v[148:151], v[4:7]\n"
  "ds_read_b64_tr_b16 a[226:227], v222 offset:33792\n"
  "v_mul_f32 v66, v66, v135\n"
  "v_mul_f32 v67, v67, v135\n"
  "v_cvt_pk_bf16_f32 v64, v64, v65\n"
  "v_cvt_pk_bf16_f32 v65, v66, v67\n"
  "v_mfma_f32_16x16x32_bf16 v[8:11], a[240:243], v[148:151], v[8:11]\n"
  "s_waitcnt lgkmcnt(0)\n"
  "ds_write2st64_b64 v192, v[0:1], v[64:65] offset0:0 offset1:4\n"
  "v_mfma_f32_16x16x32_bf16 v[8:11], a[244:247], v[160:163], v[8:11]\n"
  "s_barrier\n"
  "ds_read_b64_tr_b16 a[228:229], v230 offset:32768\n"
  "v_mfma_f32_16x16x32_bf16 v[72:75], a[244:247], v[164:167], v[72:75]\n"
  "ds_read_b64_tr_b16 a[230:231], v230 offset:33792\n"
  "v_mul_f32 v4, v4, v134\n"
  "v_mul_f32 v5, v5, v134\n"
  "v_mul_f32 v6, v6, v134\n"
  "v_mul_f32 v7, v7, v134\n"
  "v_mfma_f32_16x16x32_bf16 v[72:75], a[240:243], v[152:155], v[72:75]\n"
  "ds_read_b64_tr_b16 a[232:233], v223 offset:32768\n"
  "ds_write_b128 v191, a[4:7] offset:1024\n"
  "v_cvt_pk_bf16_f32 v4, v4, v5\n"
  "v_cvt_pk_bf16_f32 v5, v6, v7\n"
  "v_mul_f32 v68, v68, v135\n"
  "v_mul_f32 v69, v69, v135\n"
  "v_mfma_f32_16x16x32_bf16 a[8:11], a[240:243], v[156:159], a[8:11]\n"
  "ds_read_b64_tr_b16 a[234:235], v223 offset:33792\n"
  "v_mul_f32 v70, v70, v135\n"
  "v_mul_f32 v71, v71, v135\n"
  "v_cvt_pk_bf16_f32 v68, v68, v69\n"
  "v_cvt_pk_bf16_f32 v69, v70, v71\n"
  "v_mfma_f32_16x16x32_bf16 a[12:15], a[248:251], v[156:159], a[12:15]\n"
  "ds_read_b64_tr_b16 a[236:237], v231 offset:32768\n"
  "ds_write2st64_b64 v193, v[4:5], v[68:69] offset0:0 offset1:4\n"
  "v_mfma_f32_16x16x32_bf16 v[76:79], a[248:251], v[152:155], v[76:79]\n"
  "ds_read_b64_tr_b16 a[238:239], v231 offset:33792\n"
  "ds_read_b64 v[128:129], v191 offset:7168\n"
  "v_mfma_f32_16x16x32_bf16 v[76:79], a[252:255], v[164:167], v[76:79]\n"
  "ds_read_b64_tr_b16 a[240:241], v216 offset:33024\n"
  "v_mul_f32 v8, v8, v134\n"
  "v_mul_f32 v9, v9, v134\n"
  "v_mul_f32 v10, v10, v134\n"
  "v_mul_f32 v11, v11, v134\n"
  "v_mfma_f32_16x16x32_bf16 v[12:15], a[252:255], v[160:163], v[12:15]\n"
  "ds_read_b64_tr_b16 a[242:243], v216 offset:34048\n"
  "ds_write_b128 v191, a[8:11] offset:2048\n"
  "v_cvt_pk_bf16_f32 v8, v8, v9\n"
  "v_cvt_pk_bf16_f32 v9, v10, v11\n"
  "v_mul_f32 v72, v72, v135\n"
  "v_mul_f32 v73, v73, v135\n"
  "v_mfma_f32_16x16x32_bf16 v[12:15], a[248:251], v[148:151], v[12:15]\n"
  "v_mul_f32 v74, v74, v135\n"
  "v_mul_f32 v75, v75, v135\n"
  "v_cvt_pk_bf16_f32 v72, v72, v73\n"
  "v_cvt_pk_bf16_f32 v73, v74, v75\n"
  "v_mfma_f32_16x16x32_bf16 v[16:19], a[176:179], v[148:151], v[16:19]\n"
  "ds_read_b64_tr_b16 a[244:245], v224 offset:33024\n"
  "ds_write2st64_b64 v194, v[8:9], v[72:73] offset0:0 offset1:4\n"
  "v_mfma_f32_16x16x32_bf16 v[16:19], a[180:183], v[160:163], v[16:19]\n"
  "ds_read_b64_tr_b16 a[246:247], v224 offset:34048\n"
  "v_mfma_f32_16x16x32_bf16 v[80:83], a[180:183], v[164:167], v[80:83]\n"
  "ds_read_b64_tr_b16 a[248:249], v217 offset:33024\n"
  "v_mul_f32 v12, v12, v134\n"
  "v_mul_f32 v13, v13, v134\n"
  "v_mul_f32 v14, v14, v134\n"
  "v_mul_f32 v15, v15, v134\n"
  "v_mfma_f32_16x16x32_bf16 v[80:83], a[176:179], v[152:155], v[80:83]\n"
  "ds_read_b64_tr_b16 a[250:251], v217 offset:34048\n"
  "ds_write_b128 v191, a[12:15] offset:3072\n"
  "v_cvt_pk_bf16_f32 v12, v12, v13\n"
  "v_cvt_pk_bf16_f32 v13, v14, v15\n"
  "v_mul_f32 v76, v76, v135\n"
  "v_mul_f32 v77, v77, v135\n"
  "v_mfma_f32_16x16x32_bf16 a[16:19], a[176:179], v[156:159], a[16:19]\n"
  "ds_read_b64_tr_b16 a[252:253], v225 offset:33024\n"
  "v_mul_f32 v78, v78, v135\n"
  "v_mul_f32 v79, v79, v135\n"
  "v_cvt_pk_bf16_f32 v76, v76, v77\n"
  "v_cvt_pk_bf16_f32 v77, v78, v79\n"
  "v_mfma_f32_16x16x32_bf16 a[20:23], a[184:187], v[156:159], a[20:23]\n"
  "ds_read_b64_tr_b16 a[254:255], v225 offset:34048\n"
  "ds_write2st64_b64 v195, v[12:13], v[76:77] offset0:0 offset1:4\n"
  "v_mfma_f32_16x16x32_bf16 v[84:87], a[184:187], v[152:155], v[84:87]\n"
  "ds_read_b64_tr_b16 a[176:177], v218 offset:33024\n"
  "ds_read_b128 v[0:3], v196 offset:0\n"
  "ds_read_b128 v[4:7], v196 offset:1024\n"
  "v_mfma_f32_16x16x32_bf16 v[84:87], a[188:191], v[164:167], v[84:87]\n"
  "ds_read_b128 v[64:67], v196 offset:2048\n"
  "ds_read_b128 v[68:71], v196 offset:3072\n"
  "v_mul_f32 v16, v16, v134\n"
  "v_mul_f32 v17, v17, v134\n"
  "v_mul_f32 v18, v18, v134\n"
  "v_mul_f32 v19, v19, v134\n"
  "v_mfma_f32_16x16x32_bf16 v[20:23], a[188:191], v[160:163], v[20:23]\n"
  "ds_write_b128 v191, a[16:19] offset:4096\n"
  "v_cvt_pk_bf16_f32 v16, v16, v17\n"
  "v_cvt_pk_bf16_f32 v17, v18, v19\n"
  "v_mul_f32 v80, v80, v135\n"
  "v_mul_f32 v81, v81, v135\n"
  "v_mfma_f32_16x16x32_bf16 v[20:23], a[184:187], v[148:151], v[20:23]\n"
  "ds_read_b64_tr_b16 a[178:179], v218 offset:34048\n"
  "v_mul_f32 v82, v82, v135\n"
  "v_mul_f32 v83, v83, v135\n"
  "v_cvt_pk_bf16_f32 v80, v80, v81\n"
  "v_cvt_pk_bf16_f32 v81, v82, v83\n"
  "v_mfma_f32_16x16x32_bf16 v[24:27], a[224:227], v[148:151], v[24:27]\n"
  "s_waitcnt lgkmcnt(13)\n"
  "ds_read_b64_tr_b16 a[180:181], v226 offset:33024\n"
  "ds_write2st64_b64 v192, v[16:17], v[80:81] offset0:0 offset1:4\n"
  "v_max_f32 v138, v130, v128\n"
  "v_sub_f32 v132, v130, v138\n"
  "v_sub_f32 v133, v128, v138\n"
  "v_exp_f32 v132, v132\n"
  "v_mfma_f32_16x16x32_bf16 v[24:27], a[228:231], v[160:163], v[24:27]\n"
  "ds_read_b64_tr_b16 a[182:183], v226 offset:34048\n"
  "s_waitcnt lgkmcnt(8)\n"
  "buffer_store_dwordx4 v[0:3], v188, s[52:55], 0 offen offset:0 nt\n"
  "v_exp_f32 v133, v133\n"
  "v_add_u32 v141, s94, v192\n"
  "v_add_u32 v142, s94, v193\n"
  "v_mul_f32 v136, v131, v132\n"
  "v_mfma_f32_16x16x32_bf16 v[88:91], a[228:231], v[164:167], v[88:91]\n"
  "ds_read_b64_tr_b16 a[184:185], v219 offset:33024\n"
  "v_fma_f32 v136, v129, v133, v136\n"
  "v_rcp_f32 v136, v136\n"
  "v_add_u32 v143, s94, v194\n"
  "v_add_u32 v144, s94, v195\n"
  "v_mfma_f32_16x16x32_bf16 v[88:91], a[224:227], v[152:155], v[88:91]\n"
  "ds_read_b64_tr_b16 a[186:187], v219 offset:34048\n"
  "ds_write_b128 v191, a[20:23] offset:5120\n"
  "v_add_u32 v145, s94, v196\n"
  "v_mul_f32 v132, v132, v136\n"
  "v_mul_f32 v133, v133, v136\n"
  "v_mul_f32 v20, v20, v134\n"
  "v_mfma_f32_16x16x32_bf16 a[24:27], a[224:227], v[156:159], a[24:27]\n"
  "ds_read_b64_tr_b16 a[188:189], v227 offset:33024\n"
  "s_waitcnt lgkmcnt(11)\n"
  "buffer_store_dwordx4 v[4:7], v188, s[52:55], s82 offen offset:0 nt\n"
  "v_mul_f32 v21, v21, v134\n"
  "v_mul_f32 v22, v22, v134\n"
  "v_mul_f32 v23, v23, v134\n"
  "v_cvt_pk_bf16_f32 v20, v20, v21\n"
  "v_mfma_f32_16x16x32_bf16 a[28:31], a[232:235], v[156:159], a[28:31]\n"
  "ds_read_b64_tr_b16 a[190:191], v227 offset:34048\n"
  "v_cvt_pk_bf16_f32 v21, v22, v23\n"
  "v_mul_f32 v84, v84, v135\n"
  "v_mul_f32 v85, v85, v135\n"
  "v_mul_f32 v86, v86, v135\n"
  "v_mfma_f32_16x16x32_bf16 v[92:95], a[232:235], v[152:155], v[92:95]\n"
  "ds_read_b64_tr_b16 a[224:225], v220 offset:33024\n"
  "s_waitcnt lgkmcnt(12)\n"
  "buffer_store_dwordx4 v[64:67], v188, s[52:55], s81 offen offset:0 nt\n"
  "v_mul_f32 v87, v87, v135\n"
  "v_cvt_pk_bf16_f32 v84, v84, v85\n"
  "v_cvt_pk_bf16_f32 v85, v86, v87\n"
  "v_mfma_f32_16x16x32_bf16 v[92:95], a[236:239], v[164:167], v[92:95]\n"
  "ds_write2st64_b64 v193, v[20:21], v[84:85] offset0:0 offset1:4\n"
  "v_mul_f32 v24, v24, v134\n"
  "v_mul_f32 v25, v25, v134\n"
  "v_mul_f32 v26, v26, v134\n"
  "v_mul_f32 v27, v27, v134\n"
  "v_mfma_f32_16x16x32_bf16 v[28:31], a[236:239], v[160:163], v[28:31]\n"
  "ds_read_b64_tr_b16 a[226:227], v220 offset:34048\n"
  "ds_read_b64_tr_b16 a[228:229], v228 offset:33024\n"
  "ds_write_b128 v191, a[24:27] offset:6144\n"
  "v_cvt_pk_bf16_f32 v24, v24, v25\n"
  "v_cvt_pk_bf16_f32 v25, v26, v27\n"
  "v_mul_f32 v88, v88, v135\n"
  "v_mul_f32 v89, v89, v135\n"
  "v_mfma_f32_16x16x32_bf16 v[28:31], a[232:235], v[148:151], v[28:31]\n"
  "ds_read_b64_tr_b16 a[230:231], v228 offset:34048\n"
  "s_waitcnt lgkmcnt(15)\n"
  "buffer_store_dwordx4 v[68:71], v188, s[52:55], s92 offen offset:0 nt\n"
  "v_mul_f32 v90, v90, v135\n"
  "v_mul_f32 v91, v91, v135\n"
  "v_cvt_pk_bf16_f32 v88, v88, v89\n"
  "v_cvt_pk_bf16_f32 v89, v90, v91\n"
  "v_mfma_f32_16x16x32_bf16 v[32:35], a[240:243], v[148:151], v[32:35]\n"
  "ds_write2st64_b64 v194, v[24:25], v[88:89] offset0:0 offset1:4\n"
  "v_mfma_f32_16x16x32_bf16 v[32:35], a[244:247], v[160:163], v[32:35]\n"
  "ds_read_b64_tr_b16 a[232:233], v221 offset:33024\n"
  "v_mfma_f32_16x16x32_bf16 v[96:99], a[244:247], v[164:167], v[96:99]\n"
  "ds_read_b64_tr_b16 a[234:235], v221 offset:34048\n"
  "ds_read_b64_tr_b16 a[236:237], v229 offset:33024\n"
  "v_mul_f32 v28, v28, v134\n"
  "v_mul_f32 v29, v29, v134\n"
  "v_mul_f32 v30, v30, v134\n"
  "v_mul_f32 v31, v31, v134\n"
  "v_mfma_f32_16x16x32_bf16 v[96:99], a[240:243], v[152:155], v[96:99]\n"
  "ds_write_b128 v191, a[28:31] offset:7168\n"
  "v_cvt_pk_bf16_f32 v28, v28, v29\n"
  "v_cvt_pk_bf16_f32 v29, v30, v31\n"
  "v_mul_f32 v92, v92, v135\n"
  "v_mul_f32 v93, v93, v135\n"
  "v_mfma_f32_16x16x32_bf16 a[32:35], a[240:243], v[156:159], a[32:35]\n"
  "s_waitcnt lgkmcnt(11)\n"
  "ds_read_b64_tr_b16 a[238:239], v229 offset:34048\n"
  "v_mul_f32 v94, v94, v135\n"
  "v_mul_f32 v95, v95, v135\n"
  "v_cvt_pk_bf16_f32 v92, v92, v93\n"
  "v_cvt_pk_bf16_f32 v93, v94, v95\n"
  "v_mfma_f32_16x16x32_bf16 a[36:39], a[248:251], v[156:159], a[36:39]\n"
  "ds_read_b64_tr_b16 a[240:241], v222 offset:33024\n"
  "ds_write2st64_b64 v195, v[28:29], v[92:93] offset0:0 offset1:4\n"
  "v_mfma_f32_16x16x32_bf16 v[100:103], a[248:251], v[152:155], v[100:103]\n"
  "ds_read_b128 v[16:19], v196 offset:0\n"
  "ds_read_b128 v[20:23], v196 offset:1024\n"
  "ds_read_b128 v[80:83], v196 offset:2048\n"
  "v_mfma_f32_16x16x32_bf16 v[100:103], a[252:255], v[164:167], v[100:103]\n"
  "ds_read_b64_tr_b16 a[242:243], v222 offset:34048\n"
  "ds_read_b64_tr_b16 a[244:245], v230 offset:33024\n"
  "ds_read_b128 v[84:87], v196 offset:3072\n"
  "v_mul_f32 v32, v32, v134\n"
  "v_mul_f32 v33, v33, v134\n"
  "v_mul_f32 v34, v34, v134\n"
  "v_mul_f32 v35, v35, v134\n"
  "v_mfma_f32_16x16x32_bf16 v[36:39], a[252:255], v[160:163], v[36:39]\n"
  "v_cvt_pk_bf16_f32 v32, v32, v33\n"
  "v_cvt_pk_bf16_f32 v33, v34, v35\n"
  "v_mul_f32 v96, v96, v135\n"
  "v_mul_f32 v97, v97, v135\n"
  "v_mfma_f32_16x16x32_bf16 v[36:39], a[248:251], v[148:151], v[36:39]\n"
  "s_waitcnt lgkmcnt(0)\n"
  "ds_read_b64_tr_b16 a[246:247], v230 offset:34048\n"
  "v_mul_f32 v98, v98, v135\n"
  "v_mul_f32 v99, v99, v135\n"
  "v_cvt_pk_bf16_f32 v96, v96, v97\n"
  "v_cvt_pk_bf16_f32 v97, v98, v99\n"
  "v_mfma_f32_16x16x32_bf16 v[40:43], a[176:179], v[148:151], v[40:43]\n"
  "s_barrier\n"
  "ds_read_b64_tr_b16 a[248:249], v223 offset:33024\n"
  "ds_read_b64_tr_b16 a[250:251], v223 offset:34048\n"
  "ds_write2st64_b64 v192, v[32:33], v[96:97] offset0:0 offset1:4\n"
  "v_mfma_f32_16x16x32_bf16 v[40:43], a[180:183], v[160:163], v[40:43]\n"
  "ds_read_b128 v[8:11], v137 offset:0\n"
  "ds_read_b128 v[12:15], v137 offset:1024\n"
  "ds_read_b128 v[72:75], v137 offset:2048\n"
  "buffer_store_dwordx4 v[16:19], v188, s[52:55], 0 offen offset:128 nt\n"
  "v_mfma_f32_16x16x32_bf16 v[104:107], a[180:183], v[164:167], v[104:107]\n"
  "ds_read_b64_tr_b16 a[252:253], v231 offset:33024\n"
  "ds_read_b128 v[76:79], v137 offset:3072\n"
  "ds_read_b128 v[24:27], v137 offset:4096\n"
  "v_mul_f32 v36, v36, v134\n"
  "v_mul_f32 v37, v37, v134\n"
  "v_mul_f32 v38, v38, v134\n"
  "v_mul_f32 v39, v39, v134\n"
  "v_cvt_pk_bf16_f32 v36, v36, v37\n"
  "v_cvt_pk_bf16_f32 v37, v38, v39\n"
  "v_mfma_f32_16x16x32_bf16 v[104:107], a[176:179], v[152:155], v[104:107]\n"
  "ds_read_b128 v[28:31], v137 offset:5120\n"
  "ds_read_b128 v[88:91], v137 offset:6144\n"
  "ds_read_b128 v[92:95], v137 offset:7168\n"
  "buffer_store_dwordx4 v[20:23], v188, s[52:55], s82 offen offset:128 nt\n"
  "v_mul_f32 v100, v100, v135\n"
  "v_mul_f32 v101, v101, v135\n"
  "v_mul_f32 v102, v102, v135\n"
  "v_mul_f32 v103, v103, v135\n"
  "v_cvt_pk_bf16_f32 v100, v100, v101\n"
  "v_cvt_pk_bf16_f32 v101, v102, v103\n"
  "v_mfma_f32_16x16x32_bf16 a[40:43], a[176:179], v[156:159], a[40:43]\n"
  "ds_read_b64_tr_b16 a[254:255], v231 offset:34048\n"
  "ds_write2st64_b64 v193, v[36:37], v[100:101] offset0:0 offset1:4\n"
  "v_accvgpr_read_b32 v168, a32\n"
  "v_accvgpr_read_b32 v169, a33\n"
  "v_accvgpr_read_b32 v170, a34\n"
  "v_accvgpr_read_b32 v171, a35\n"
  "v_mul_f32 v168, v168, v132\n"
  "v_mul_f32 v169, v169, v132\n"
  "v_mfma_f32_16x16x32_bf16 a[44:47], a[184:187], v[156:159], a[44:47]\n"
  "buffer_store_dwordx4 v[80:83], v188, s[52:55], s81 offen offset:128 nt\n"
  "v_mul_f32 v170, v170, v132\n"
  "v_mul_f32 v171, v171, v132\n"
  "s_waitcnt lgkmcnt(10)\n"
  "v_fma_f32 v168, v8, v133, v168\n"
  "v_fma_f32 v169, v9, v133, v169\n"
  "v_fma_f32 v170, v10, v133, v170\n"
  "v_fma_f32 v171, v11, v133, v171\n"
  "v_mfma_f32_16x16x32_bf16 v[108:111], a[184:187], v[152:155], v[108:111]\n"
  "v_cvt_pk_bf16_f32 v168, v168, v169\n"
  "v_cvt_pk_bf16_f32 v169, v170, v171\n"
  "v_mfma_f32_16x16x32_bf16 v[108:111], a[188:191], v[164:167], v[108:111]\n"
  "ds_write_b64 v141, v[168:169] offset:0\n"
  "v_mul_f32 v40, v40, v134\n"
  "v_mul_f32 v41, v41, v134\n"
  "v_mul_f32 v42, v42, v134\n"
  "v_mul_f32 v43, v43, v134\n"
  "v_cvt_pk_bf16_f32 v40, v40, v41\n"
  "v_cvt_pk_bf16_f32 v41, v42, v43\n"
  "v_mfma_f32_16x16x32_bf16 v[44:47], a[188:191], v[160:163], v[44:47]\n"
  "buffer_store_dwordx4 v[84:87], v188, s[52:55], s92 offen offset:128 nt\n"
  "v_mul_f32 v104, v104, v135\n"
  "v_mul_f32 v105, v105, v135\n"
  "v_mul_f32 v106, v106, v135\n"
  "v_mul_f32 v107, v107, v135\n"
  "v_cvt_pk_bf16_f32 v104, v104, v105\n"
  "v_cvt_pk_bf16_f32 v105, v106, v107\n"
  "v_mfma_f32_16x16x32_bf16 v[44:47], a[184:187], v[148:151], v[44:47]\n"
  "ds_write2st64_b64 v194, v[40:41], v[104:105] offset0:0 offset1:4\n"
  "v_accvgpr_read_b32 v172, a36\n"
  "v_accvgpr_read_b32 v173, a37\n"
  "v_accvgpr_read_b32 v174, a38\n"
  "v_accvgpr_read_b32 v175, a39\n"
  "v_mul_f32 v172, v172, v132\n"
  "v_mul_f32 v173, v173, v132\n"
  "v_mfma_f32_16x16x32_bf16 v[48:51], a[224:227], v[148:151], v[48:51]\n"
  "v_mul_f32 v174, v174, v132\n"
  "v_mul_f32 v175, v175, v132\n"
  "s_waitcnt lgkmcnt(11)\n"
  "v_fma_f32 v172, v12, v133, v172\n"
  "v_fma_f32 v173, v13, v133, v173\n"
  "v_fma_f32 v174, v14, v133, v174\n"
  "v_fma_f32 v175, v15, v133, v175\n"
  "v_mfma_f32_16x16x32_bf16 v[48:51], a[228:231], v[160:163], v[48:51]\n"
  "v_cvt_pk_bf16_f32 v172, v172, v173\n"
  "v_cvt_pk_bf16_f32 v173, v174, v175\n"
  "v_mfma_f32_16x16x32_bf16 v[112:115], a[228:231], v[164:167], v[112:115]\n"
  "ds_write_b64 v142, v[172:173] offset:0\n"
  "v_mul_f32 v44, v44, v134\n"
  "v_mul_f32 v45, v45, v134\n"
  "v_mul_f32 v46, v46, v134\n"
  "v_mul_f32 v47, v47, v134\n"
  "v_cvt_pk_bf16_f32 v44, v44, v45\n"
  "v_cvt_pk_bf16_f32 v45, v46, v47\n"
  "v_mfma_f32_16x16x32_bf16 v[112:115], a[224:227], v[152:155], v[112:115]\n"
  "v_mul_f32 v108, v108, v135\n"
  "v_mul_f32 v109, v109, v135\n"
  "v_mul_f32 v110, v110, v135\n"
  "v_mul_f32 v111, v111, v135\n"
  "v_cvt_pk_bf16_f32 v108, v108, v109\n"
  "v_cvt_pk_bf16_f32 v109, v110, v111\n"
  "v_mfma_f32_16x16x32_bf16 a[48:51], a[224:227], v[156:159], a[48:51]\n"
  "ds_write2st64_b64 v195, v[44:45], v[108:109] offset0:0 offset1:4\n"
  "v_accvgpr_read_b32 v176, a40\n"
  "v_accvgpr_read_b32 v177, a41\n"
  "v_accvgpr_read_b32 v178, a42\n"
  "v_accvgpr_read_b32 v179, a43\n"
  "v_mul_f32 v176, v176, v132\n"
  "v_mul_f32 v177, v177, v132\n"
  "v_mfma_f32_16x16x32_bf16 a[52:55], a[232:235], v[156:159], a[52:55]\n"
  "ds_read_b128 v[32:35], v196 offset:0\n"
  "ds_read_b128 v[36:39], v196 offset:1024\n"
  "ds_read_b128 v[96:99], v196 offset:2048\n"
  "v_mul_f32 v178, v178, v132\n"
  "v_mul_f32 v179, v179, v132\n"
  "s_waitcnt lgkmcnt(15)\n"
  "v_fma_f32 v176, v72, v133, v176\n"
  "v_fma_f32 v177, v73, v133, v177\n"
  "v_fma_f32 v178, v74, v133, v178\n"
  "v_fma_f32 v179, v75, v133, v179\n"
  "v_mfma_f32_16x16x32_bf16 v[116:119], a[232:235], v[152:155], v[116:119]\n"
  "ds_read_b128 v[100:103], v196 offset:3072\n"
  "v_cvt_pk_bf16_f32 v176, v176, v177\n"
  "v_cvt_pk_bf16_f32 v177, v178, v179\n"
  "v_mfma_f32_16x16x32_bf16 v[116:119], a[236:239], v[164:167], v[116:119]\n"
  "ds_write_b64 v143, v[176:177] offset:0\n"
  "v_mul_f32 v48, v48, v134\n"
  "v_mul_f32 v49, v49, v134\n"
  "v_mul_f32 v50, v50, v134\n"
  "v_mul_f32 v51, v51, v134\n"
  "v_cvt_pk_bf16_f32 v48, v48, v49\n"
  "v_cvt_pk_bf16_f32 v49, v50, v51\n"
  "v_mfma_f32_16x16x32_bf16 v[52:55], a[236:239], v[160:163], v[52:55]\n"
  "v_mul_f32 v112, v112, v135\n"
  "v_mul_f32 v113, v113, v135\n"
  "v_mul_f32 v114, v114, v135\n"
  "v_mul_f32 v115, v115, v135\n"
  "v_cvt_pk_bf16_f32 v112, v112, v113\n"
  "v_cvt_pk_bf16_f32 v113, v114, v115\n"
  "v_mfma_f32_16x16x32_bf16 v[52:55], a[232:235], v[148:151], v[52:55]\n"
  "ds_write2st64_b64 v192, v[48:49], v[112:113] offset0:0 offset1:4\n"
  "v_accvgpr_read_b32 v180, a44\n"
  "v_accvgpr_read_b32 v181, a45\n"
  "v_accvgpr_read_b32 v182, a46\n"
  "v_accvgpr_read_b32 v183, a47\n"
  "v_mul_f32 v180, v180, v132\n"
  "v_mul_f32 v181, v181, v132\n"
  "v_mfma_f32_16x16x32_bf16 v[56:59], a[240:243], v[148:151], v[56:59]\n"
  "s_waitcnt lgkmcnt(8)\n"
  "s_waitcnt lgkmcnt(5)\n"
  "buffer_store_dwordx4 v[32:35], v188, s[52:55], 0 offen offset:256 nt\n"
  "v_mul_f32 v182, v182, v132\n"
  "v_mul_f32 v183, v183, v132\n"
  "v_fma_f32 v180, v76, v133, v180\n"
  "v_fma_f32 v181, v77, v133, v181\n"
  "v_fma_f32 v182, v78, v133, v182\n"
  "v_fma_f32 v183, v79, v133, v183\n"
  "v_mfma_f32_16x16x32_bf16 v[56:59], a[244:247], v[160:163], v[56:59]\n"
  "v_cvt_pk_bf16_f32 v180, v180, v181\n"
  "v_cvt_pk_bf16_f32 v181, v182, v183\n"
  "v_mfma_f32_16x16x32_bf16 v[120:123], a[244:247], v[164:167], v[120:123]\n"
  "ds_write_b64 v144, v[180:181] offset:0\n"
  "v_mul_f32 v52, v52, v134\n"
  "v_mul_f32 v53, v53, v134\n"
  "v_mul_f32 v54, v54, v134\n"
  "v_mul_f32 v55, v55, v134\n"
  "v_cvt_pk_bf16_f32 v52, v52, v53\n"
  "v_cvt_pk_bf16_f32 v53, v54, v55\n"
  "v_mfma_f32_16x16x32_bf16 v[120:123], a[240:243], v[152:155], v[120:123]\n"
  "ds_read_b128 v[244:247], v145 offset:0\n"
  "ds_read_b128 v[248:251], v145 offset:1024\n"
  "s_waitcnt lgkmcnt(7)\n"
  "buffer_store_dwordx4 v[36:39], v188, s[52:55], s82 offen offset:256 nt\n"
  "v_mul_f32 v116, v116, v135\n"
  "v_mul_f32 v117, v117, v135\n"
  "v_mul_f32 v118, v118, v135\n"
  "v_mul_f32 v119, v119, v135\n"
  "v_cvt_pk_bf16_f32 v116, v116, v117\n"
  "v_cvt_pk_bf16_f32 v117, v118, v119\n"
  "v_mfma_f32_16x16x32_bf16 a[56:59], a[240:243], v[156:159], a[56:59]\n"
  "ds_write2st64_b64 v193, v[52:53], v[116:117] offset0:0 offset1:4\n"
  "v_accvgpr_read_b32 v168, a48\n"
  "v_accvgpr_read_b32 v169, a49\n"
  "v_accvgpr_read_b32 v170, a50\n"
  "v_accvgpr_read_b32 v171, a51\n"
  "v_mul_f32 v168, v168, v132\n"
  "v_mul_f32 v169, v169, v132\n"
  "v_mfma_f32_16x16x32_bf16 a[60:63], a[248:251], v[156:159], a[60:63]\n"
  "s_waitcnt lgkmcnt(7)\n"
  "buffer_store_dwordx4 v[96:99], v188, s[52:55], s81 offen offset:256 nt\n"
  "v_mul_f32 v170, v170, v132\n"
  "v_mul_f32 v171, v171, v132\n"
  "v_fma_f32 v168, v24, v133, v168\n"
  "v_fma_f32 v169, v25, v133, v169\n"
  "v_fma_f32 v170, v26, v133, v170\n"
  "v_fma_f32 v171, v27, v133, v171\n"
  "v_mfma_f32_16x16x32_bf16 v[124:127], a[248:251], v[152:155], v[124:127]\n"
  "v_cvt_pk_bf16_f32 v168, v168, v169\n"
  "v_cvt_pk_bf16_f32 v169, v170, v171\n"
  "v_mfma_f32_16x16x32_bf16 v[124:127], a[252:255], v[164:167], v[124:127]\n"
  "ds_write_b64 v141, v[168:169] offset:0\n"
  "v_mul_f32 v56, v56, v134\n"
  "v_mul_f32 v57, v57, v134\n"
  "v_mul_f32 v58, v58, v134\n"
  "v_mul_f32 v59, v59, v134\n"
  "v_cvt_pk_bf16_f32 v56, v56, v57\n"
  "v_cvt_pk_bf16_f32 v57, v58, v59\n"
  "v_mfma_f32_16x16x32_bf16 v[60:63], a[252:255], v[160:163], v[60:63]\n"
  "s_waitcnt lgkmcnt(7)\n"
  "buffer_store_dwordx4 v[100:103], v188, s[52:55], s92 offen offset:256 nt\n"
  "v_mul_f32 v120, v120, v135\n"
  "v_mul_f32 v121, v121, v135\n"
  "v_mul_f32 v122, v122, v135\n"
  "v_mul_f32 v123, v123, v135\n"
  "v_cvt_pk_bf16_f32 v120, v120, v121\n"
  "v_cvt_pk_bf16_f32 v121, v122, v123\n"
  "v_mfma_f32_16x16x32_bf16 v[60:63], a[248:251], v[148:151], v[60:63]\n"
  "ds_write2st64_b64 v194, v[56:57], v[120:121] offset0:0 offset1:4\n"
  "v_accvgpr_read_b32 v172, a52\n"
  "v_accvgpr_read_b32 v173, a53\n"
  "v_accvgpr_read_b32 v174, a54\n"
  "v_accvgpr_read_b32 v175, a55\n"
  "v_mul_f32 v172, v172, v132\n"
  "v_mul_f32 v173, v173, v132\n"
  "s_waitcnt lgkmcnt(4)\n"
  "buffer_store_dwordx4 v[244:247], v189, s[52:55], 0 offen offset:0 nt\n"
  "v_mul_f32 v174, v174, v132\n"
  "v_mul_f32 v175, v175, v132\n"
  "v_fma_f32 v172, v28, v133, v172\n"
  "v_fma_f32 v173, v29, v133, v173\n"
  "v_fma_f32 v174, v30, v133, v174\n"
  "v_fma_f32 v175, v31, v133, v175\n"
  "v_cvt_pk_bf16_f32 v172, v172, v173\n"
  "v_cvt_pk_bf16_f32 v173, v174, v175\n"
  "ds_write_b64 v142, v[172:173] offset:0\n"
  "v_mul_f32 v60, v60, v134\n"
  "v_mul_f32 v61, v61, v134\n"
  "v_mul_f32 v62, v62, v134\n"
  "v_mul_f32 v63, v63, v134\n"
  "v_cvt_pk_bf16_f32 v60, v60, v61\n"
  "v_cvt_pk_bf16_f32 v61, v62, v63\n"
  "s_waitcnt lgkmcnt(4)\n"
  "buffer_store_dwordx4 v[248:251], v189, s[52:55], s82 offen offset:0 nt\n"
  "v_mul_f32 v124, v124, v135\n"
  "v_mul_f32 v125, v125, v135\n"
  "v_mul_f32 v126, v126, v135\n"
  "v_mul_f32 v127, v127, v135\n"
  "v_cvt_pk_bf16_f32 v124, v124, v125\n"
  "v_cvt_pk_bf16_f32 v125, v126, v127\n"
  "ds_write2st64_b64 v195, v[60:61], v[124:125] offset0:0 offset1:4\n"
  "v_accvgpr_read_b32 v176, a56\n"
  "v_accvgpr_read_b32 v177, a57\n"
  "v_accvgpr_read_b32 v178, a58\n"
  "v_accvgpr_read_b32 v179, a59\n"
  "v_mul_f32 v176, v176, v132\n"
  "v_mul_f32 v177, v177, v132\n"
  "ds_read_b128 v[48:51], v196 offset:0\n"
  "ds_read_b128 v[52:55], v196 offset:1024\n"
  "ds_read_b128 v[112:115], v196 offset:2048\n"
  "v_mul_f32 v178, v178, v132\n"
  "v_mul_f32 v179, v179, v132\n"
  "v_fma_f32 v176, v88, v133, v176\n"
  "v_fma_f32 v177, v89, v133, v177\n"
  "v_fma_f32 v178, v90, v133, v178\n"
  "v_fma_f32 v179, v91, v133, v179\n"
  "ds_read_b128 v[116:119], v196 offset:3072\n"
  "v_cvt_pk_bf16_f32 v176, v176, v177\n"
  "v_cvt_pk_bf16_f32 v177, v178, v179\n"
  "v_accvgpr_read_b32 v180, a60\n"
  "v_accvgpr_read_b32 v181, a61\n"
  "v_accvgpr_read_b32 v182, a62\n"
  "v_accvgpr_read_b32 v183, a63\n"
  "ds_write_b64 v143, v[176:177] offset:0\n"
  "v_mul_f32 v180, v180, v132\n"
  "v_mul_f32 v181, v181, v132\n"
  "v_mul_f32 v182, v182, v132\n"
  "v_mul_f32 v183, v183, v132\n"
  "v_fma_f32 v180, v92, v133, v180\n"
  "v_fma_f32 v181, v93, v133, v181\n"
  "v_fma_f32 v182, v94, v133, v182\n"
  "v_fma_f32 v183, v95, v133, v183\n"
  "v_cvt_pk_bf16_f32 v180, v180, v181\n"
  "v_cvt_pk_bf16_f32 v181, v182, v183\n"
  "ds_write_b64 v144, v[180:181] offset:0\n"
  "ds_read_b128 v[244:247], v145 offset:0\n"
  "ds_read_b128 v[248:251], v145 offset:1024\n"
  "s_waitcnt lgkmcnt(7)\n"
  "buffer_store_dwordx4 v[48:51], v188, s[52:55], 0 offen offset:384 nt\n"
  "s_waitcnt lgkmcnt(6)\n"
  "buffer_store_dwordx4 v[52:55], v188, s[52:55], s82 offen offset:384 nt\n"
  "s_waitcnt lgkmcnt(5)\n"
  "buffer_store_dwordx4 v[112:115], v188, s[52:55], s81 offen offset:384 nt\n"
  "s_waitcnt lgkmcnt(4)\n"
  "buffer_store_dwordx4 v[116:119], v188, s[52:55], s92 offen offset:384 nt\n"
  "s_waitcnt lgkmcnt(1)\n"
  "buffer_store_dwordx4 v[244:247], v189, s[52:55], 0 offen offset:128 nt\n"
  "s_waitcnt lgkmcnt(0)\n"
  "buffer_store_dwordx4 v[248:251], v189, s[52:55], s82 offen offset:128 nt\n"
  "s_endpgm\n"
  ".Lattn_CODE1:\n"
  ".Lattn_RARE0:\n"
  "s_nop 7\n"
  "s_mov_b32 s84, s90\n"
  "s_mov_b32 s85, s91\n"
  "s_mov_b32 s86, 1048576\n"
  "s_mov_b32 s87, 0x20000\n"
  "s_sub_u32 s64, s58, 1\n"
  "s_lshl_b32 s64, s64, 15\n"
  "s_add_u32 s64, s64, s77\n"
  "s_and_b32 s64, s64, 0x7ffff\n"
  "s_or_b32 s64, s64, s78\n"
  "s_lshl_b32 s65, s70, 11\n"
  "v_mbcnt_lo_u32_b32 v244, -1, 0\n"
  "v_mbcnt_hi_u32_b32 v244, -1, v244\n"
  "v_and_b32 v245, 15, v244\n"
  "v_and_b32 v247, 9, v245\n"
  "v_bfe_u32 v246, v245, 1, 2\n"
  "v_lshl_add_u32 v247, v246, 4, v247\n"
  "v_lshlrev_b32 v247, 9, v247\n"
  "v_lshrrev_b32 v246, 4, v244\n"
  "v_lshl_add_u32 v247, v246, 4, v247\n"
  "v_add_u32 v247, s65, v247\n"
  "v_xor_b32 v248, 0x800, v247\n"
  "s_nop 7\n"
  "buffer_load_dwordx4 a[224:227], v247, s[84:87], s64 offen offset:0\n"
  "buffer_load_dwordx4 a[228:231], v247, s[84:87], s64 offen offset:64\n"
  "buffer_load_dwordx4 a[232:235], v247, s[84:87], s64 offen offset:128\n"
  "buffer_load_dwordx4 a[236:239], v247, s[84:87], s64 offen offset:192\n"
  "buffer_load_dwordx4 a[240:243], v247, s[84:87], s64 offen offset:256\n"
  "buffer_load_dwordx4 a[244:247], v247, s[84:87], s64 offen offset:320\n"
  "buffer_load_dwordx4 a[248:251], v247, s[84:87], s64 offen offset:384\n"
  "buffer_load_dwordx4 a[252:255], v247, s[84:87], s64 offen offset:448\n"
  "s_waitcnt vmcnt(0)\n"
  "v_mfma_f32_16x16x32_bf16 v[128:131], a[224:227], a[64:67], 0\n"
  "v_mfma_f32_16x16x32_bf16 v[128:131], a[228:231], a[68:71], v[128:131]\n"
  "v_mfma_f32_16x16x32_bf16 v[128:131], a[232:235], a[72:75], v[128:131]\n"
  "v_mfma_f32_16x16x32_bf16 v[128:131], a[236:239], a[76:79], v[128:131]\n"
  "v_mfma_f32_16x16x32_bf16 v[128:131], a[240:243], a[80:83], v[128:131]\n"
  "v_mfma_f32_16x16x32_bf16 v[128:131], a[244:247], a[84:87], v[128:131]\n"
  "v_mfma_f32_16x16x32_bf16 v[128:131], a[248:251], a[88:91], v[128:131]\n"
  "v_mfma_f32_16x16x32_bf16 v[128:131], a[252:255], a[92:95], v[128:131]\n"
  "v_mfma_f32_16x16x32_bf16 v[132:135], a[224:227], a[96:99], 0\n"
  "v_mfma_f32_16x16x32_bf16 v[132:135], a[228:231], a[100:103], v[132:135]\n"
  "v_mfma_f32_16x16x32_bf16 v[132:135], a[232:235], a[104:107], v[132:135]\n"
  "v_mfma_f32_16x16x32_bf16 v[132:135], a[236:239], a[108:111], v[132:135]\n"
  "v_mfma_f32_16x16x32_bf16 v[132:135], a[240:243], a[112:115], v[132:135]\n"
  "v_mfma_f32_16x16x32_bf16 v[132:135], a[244:247], a[116:119], v[132:135]\n"
  "v_mfma_f32_16x16x32_bf16 v[132:135], a[248:251], a[120:123], v[132:135]\n"
  "v_mfma_f32_16x16x32_bf16 v[132:135], a[252:255], a[124:127], v[132:135]\n"
  "v_mfma_f32_16x16x32_bf16 v[136:139], a[224:227], a[128:131], 0\n"
  "v_mfma_f32_16x16x32_bf16 v[136:139], a[228:231], a[132:135], v[136:139]\n"
  "v_mfma_f32_16x16x32_bf16 v[136:139], a[232:235], a[136:139], v[136:139]\n"
  "v_mfma_f32_16x16x32_bf16 v[136:139], a[236:239], a[140:143], v[136:139]\n"
  "v_mfma_f32_16x16x32_bf16 v[136:139], a[240:243], a[144:147], v[136:139]\n"
  "v_mfma_f32_16x16x32_bf16 v[136:139], a[244:247], a[148:151], v[136:139]\n"
  "v_mfma_f32_16x16x32_bf16 v[136:139], a[248:251], a[152:155], v[136:139]\n"
  "v_mfma_f32_16x16x32_bf16 v[136:139], a[252:255], a[156:159], v[136:139]\n"
  "s_nop 7\n"
  "buffer_load_dwordx4 a[224:227], v247, s[84:87], s64 offen offset:1024\n"
  "buffer_load_dwordx4 a[228:231], v247, s[84:87], s64 offen offset:1088\n"
  "buffer_load_dwordx4 a[232:235], v247, s[84:87], s64 offen offset:1152\n"
  "buffer_load_dwordx4 a[236:239], v247, s[84:87], s64 offen offset:1216\n"
  "buffer_load_dwordx4 a[240:243], v247, s[84:87], s64 offen offset:1280\n"
  "buffer_load_dwordx4 a[244:247], v247, s[84:87], s64 offen offset:1344\n"
  "buffer_load_dwordx4 a[248:251], v247, s[84:87], s64 offen offset:1408\n"
  "buffer_load_dwordx4 a[252:255], v247, s[84:87], s64 offen offset:1472\n"
  "s_waitcnt vmcnt(0)\n"
  "v_mfma_f32_16x16x32_bf16 v[176:179], a[224:227], a[128:131], 0\n"
  "v_mfma_f32_16x16x32_bf16 v[176:179], a[228:231], a[132:135], v[176:179]\n"
  "v_mfma_f32_16x16x32_bf16 v[176:179], a[232:235], a[136:139], v[176:179]\n"
  "v_mfma_f32_16x16x32_bf16 v[176:179], a[236:239], a[140:143], v[176:179]\n"
  "v_mfma_f32_16x16x32_bf16 v[176:179], a[240:243], a[144:147], v[176:179]\n"
  "v_mfma_f32_16x16x32_bf16 v[176:179], a[244:247], a[148:151], v[176:179]\n"
  "v_mfma_f32_16x16x32_bf16 v[176:179], a[248:251], a[152:155], v[176:179]\n"
  "v_mfma_f32_16x16x32_bf16 v[176:179], a[252:255], a[156:159], v[176:179]\n"
  "v_mfma_f32_16x16x32_bf16 v[172:175], a[224:227], a[96:99], 0\n"
  "v_mfma_f32_16x16x32_bf16 v[172:175], a[228:231], a[100:103], v[172:175]\n"
  "v_mfma_f32_16x16x32_bf16 v[172:175], a[232:235], a[104:107], v[172:175]\n"
  "v_mfma_f32_16x16x32_bf16 v[172:175], a[236:239], a[108:111], v[172:175]\n"
  "v_mfma_f32_16x16x32_bf16 v[172:175], a[240:243], a[112:115], v[172:175]\n"
  "v_mfma_f32_16x16x32_bf16 v[172:175], a[244:247], a[116:119], v[172:175]\n"
  "v_mfma_f32_16x16x32_bf16 v[172:175], a[248:251], a[120:123], v[172:175]\n"
  "v_mfma_f32_16x16x32_bf16 v[172:175], a[252:255], a[124:127], v[172:175]\n"
  "v_mfma_f32_16x16x32_bf16 v[168:171], a[224:227], a[64:67], 0\n"
  "v_mfma_f32_16x16x32_bf16 v[168:171], a[228:231], a[68:71], v[168:171]\n"
  "v_mfma_f32_16x16x32_bf16 v[168:171], a[232:235], a[72:75], v[168:171]\n"
  "v_mfma_f32_16x16x32_bf16 v[168:171], a[236:239], a[76:79], v[168:171]\n"
  "v_mfma_f32_16x16x32_bf16 v[168:171], a[240:243], a[80:83], v[168:171]\n"
  "v_mfma_f32_16x16x32_bf16 v[168:171], a[244:247], a[84:87], v[168:171]\n"
  "v_mfma_f32_16x16x32_bf16 v[168:171], a[248:251], a[88:91], v[168:171]\n"
  "v_mfma_f32_16x16x32_bf16 v[168:171], a[252:255], a[92:95], v[168:171]\n"
  "s_nop 7\n"
  "buffer_load_dwordx4 a[224:227], v248, s[84:87], s64 offen offset:0\n"
  "buffer_load_dwordx4 a[228:231], v248, s[84:87], s64 offen offset:64\n"
  "buffer_load_dwordx4 a[232:235], v248, s[84:87], s64 offen offset:128\n"
  "buffer_load_dwordx4 a[236:239], v248, s[84:87], s64 offen offset:192\n"
  "buffer_load_dwordx4 a[240:243], v248, s[84:87], s64 offen offset:256\n"
  "buffer_load_dwordx4 a[244:247], v248, s[84:87], s64 offen offset:320\n"
  "buffer_load_dwordx4 a[248:251], v248, s[84:87], s64 offen offset:384\n"
  "buffer_load_dwordx4 a[252:255], v248, s[84:87], s64 offen offset:448\n"
  "s_waitcnt vmcnt(0)\n"
  "v_mfma_f32_16x16x32_bf16 v[140:143], a[224:227], a[64:67], 0\n"
  "v_mfma_f32_16x16x32_bf16 v[140:143], a[228:231], a[68:71], v[140:143]\n"
  "v_mfma_f32_16x16x32_bf16 v[140:143], a[232:235], a[72:75], v[140:143]\n"
  "v_mfma_f32_16x16x32_bf16 v[140:143], a[236:239], a[76:79], v[140:143]\n"
  "v_mfma_f32_16x16x32_bf16 v[140:143], a[240:243], a[80:83], v[140:143]\n"
  "v_mfma_f32_16x16x32_bf16 v[140:143], a[244:247], a[84:87], v[140:143]\n"
  "v_mfma_f32_16x16x32_bf16 v[140:143], a[248:251], a[88:91], v[140:143]\n"
  "v_mfma_f32_16x16x32_bf16 v[140:143], a[252:255], a[92:95], v[140:143]\n"
  "v_mfma_f32_16x16x32_bf16 v[144:147], a[224:227], a[96:99], 0\n"
  "v_mfma_f32_16x16x32_bf16 v[144:147], a[228:231], a[100:103], v[144:147]\n"
  "v_mfma_f32_16x16x32_bf16 v[144:147], a[232:235], a[104:107], v[144:147]\n"
  "v_mfma_f32_16x16x32_bf16 v[144:147], a[236:239], a[108:111], v[144:147]\n"
  "v_mfma_f32_16x16x32_bf16 v[144:147], a[240:243], a[112:115], v[144:147]\n"
  "v_mfma_f32_16x16x32_bf16 v[144:147], a[244:247], a[116:119], v[144:147]\n"
  "v_mfma_f32_16x16x32_bf16 v[144:147], a[248:251], a[120:123], v[144:147]\n"
  "v_mfma_f32_16x16x32_bf16 v[144:147], a[252:255], a[124:127], v[144:147]\n"
  "s_nop 7\n"
  "buffer_load_dwordx4 a[224:227], v248, s[84:87], s64 offen offset:1024\n"
  "buffer_load_dwordx4 a[228:231], v248, s[84:87], s64 offen offset:1088\n"
  "buffer_load_dwordx4 a[232:235], v248, s[84:87], s64 offen offset:1152\n"
  "buffer_load_dwordx4 a[236:239], v248, s[84:87], s64 offen offset:1216\n"
  "buffer_load_dwordx4 a[240:243], v248, s[84:87], s64 offen offset:1280\n"
  "buffer_load_dwordx4 a[244:247], v248, s[84:87], s64 offen offset:1344\n"
  "buffer_load_dwordx4 a[248:251], v248, s[84:87], s64 offen offset:1408\n"
  "buffer_load_dwordx4 a[252:255], v248, s[84:87], s64 offen offset:1472\n"
  "s_waitcnt vmcnt(0)\n"
  "v_mfma_f32_16x16x32_bf16 v[184:187], a[224:227], a[96:99], 0\n"
  "v_mfma_f32_16x16x32_bf16 v[184:187], a[228:231], a[100:103], v[184:187]\n"
  "v_mfma_f32_16x16x32_bf16 v[184:187], a[232:235], a[104:107], v[184:187]\n"
  "v_mfma_f32_16x16x32_bf16 v[184:187], a[236:239], a[108:111], v[184:187]\n"
  "v_mfma_f32_16x16x32_bf16 v[184:187], a[240:243], a[112:115], v[184:187]\n"
  "v_mfma_f32_16x16x32_bf16 v[184:187], a[244:247], a[116:119], v[184:187]\n"
  "v_mfma_f32_16x16x32_bf16 v[184:187], a[248:251], a[120:123], v[184:187]\n"
  "v_mfma_f32_16x16x32_bf16 v[184:187], a[252:255], a[124:127], v[184:187]\n"
  "v_mfma_f32_16x16x32_bf16 v[180:183], a[224:227], a[64:67], 0\n"
  "v_mfma_f32_16x16x32_bf16 v[180:183], a[228:231], a[68:71], v[180:183]\n"
  "v_mfma_f32_16x16x32_bf16 v[180:183], a[232:235], a[72:75], v[180:183]\n"
  "v_mfma_f32_16x16x32_bf16 v[180:183], a[236:239], a[76:79], v[180:183]\n"
  "v_mfma_f32_16x16x32_bf16 v[180:183], a[240:243], a[80:83], v[180:183]\n"
  "v_mfma_f32_16x16x32_bf16 v[180:183], a[244:247], a[84:87], v[180:183]\n"
  "v_mfma_f32_16x16x32_bf16 v[180:183], a[248:251], a[88:91], v[180:183]\n"
  "v_mfma_f32_16x16x32_bf16 v[180:183], a[252:255], a[92:95], v[180:183]\n"
  "s_nop 7\n"
  "v_fma_f32 v128, v128, s56, -v232\n"
  "v_fma_f32 v129, v129, s56, -v232\n"
  "v_fma_f32 v130, v130, s56, -v232\n"
  "v_fma_f32 v131, v131, s56, -v232\n"
  "v_fma_f32 v132, v132, s56, -v233\n"
  "v_fma_f32 v133, v133, s56, -v233\n"
  "v_fma_f32 v134, v134, s56, -v233\n"
  "v_fma_f32 v135, v135, s56, -v233\n"
  "v_fma_f32 v136, v136, s56, -v234\n"
  "v_fma_f32 v137, v137, s56, -v234\n"
  "v_fma_f32 v138, v138, s56, -v234\n"
  "v_fma_f32 v139, v139, s56, -v234\n"
  "v_fma_f32 v176, v176, s56, -v234\n"
  "v_fma_f32 v177, v177, s56, -v234\n"
  "v_fma_f32 v178, v178, s56, -v234\n"
  "v_fma_f32 v179, v179, s56, -v234\n"
  "v_fma_f32 v172, v172, s56, -v233\n"
  "v_fma_f32 v173, v173, s56, -v233\n"
  "v_fma_f32 v174, v174, s56, -v233\n"
  "v_fma_f32 v175, v175, s56, -v233\n"
  "v_fma_f32 v168, v168, s56, -v232\n"
  "v_fma_f32 v169, v169, s56, -v232\n"
  "v_fma_f32 v170, v170, s56, -v232\n"
  "v_fma_f32 v171, v171, s56, -v232\n"
  "v_fma_f32 v140, v140, s56, -v232\n"
  "v_fma_f32 v141, v141, s56, -v232\n"
  "v_fma_f32 v142, v142, s56, -v232\n"
  "v_fma_f32 v143, v143, s56, -v232\n"
  "v_fma_f32 v144, v144, s56, -v233\n"
  "v_fma_f32 v145, v145, s56, -v233\n"
  "v_fma_f32 v146, v146, s56, -v233\n"
  "v_fma_f32 v147, v147, s56, -v233\n"
  "v_fma_f32 v184, v184, s56, -v233\n"
  "v_fma_f32 v185, v185, s56, -v233\n"
  "v_fma_f32 v186, v186, s56, -v233\n"
  "v_fma_f32 v187, v187, s56, -v233\n"
  "v_fma_f32 v180, v180, s56, -v232\n"
  "v_fma_f32 v181, v181, s56, -v232\n"
  "v_fma_f32 v182, v182, s56, -v232\n"
  "v_fma_f32 v183, v183, s56, -v232\n"
  "v_max_f32 v248, v128, v129\n"
  "v_max3_f32 v248, v130, v131, v248\n"
  "v_max3_f32 v248, v168, v169, v248\n"
  "v_max3_f32 v248, v170, v171, v248\n"
  "v_max3_f32 v248, v140, v141, v248\n"
  "v_max3_f32 v248, v142, v143, v248\n"
  "v_max3_f32 v248, v180, v181, v248\n"
  "v_max3_f32 v248, v182, v183, v248\n"
  "v_mov_b32 v244, v248\n"
  "s_nop 1\n"
  "v_permlane32_swap_b32 v248, v244\n"
  "v_max_f32 v248, v248, v244\n"
  "v_mov_b32 v244, v248\n"
  "s_nop 1\n"
  "v_permlane16_swap_b32 v248, v244\n"
  "v_max_f32 v248, v248, v244\n"
  "v_ceil_f32 v248, v248\n"
  "v_max_f32 v248, 0, v248\n"
  "v_sub_f32 v128, v128, v248\n"
  "v_sub_f32 v129, v129, v248\n"
  "v_sub_f32 v130, v130, v248\n"
  "v_sub_f32 v131, v131, v248\n"
  "v_sub_f32 v168, v168, v248\n"
  "v_sub_f32 v169, v169, v248\n"
  "v_sub_f32 v170, v170, v248\n"
  "v_sub_f32 v171, v171, v248\n"
  "v_sub_f32 v140, v140, v248\n"
  "v_sub_f32 v141, v141, v248\n"
  "v_sub_f32 v142, v142, v248\n"
  "v_sub_f32 v143, v143, v248\n"
  "v_sub_f32 v180, v180, v248\n"
  "v_sub_f32 v181, v181, v248\n"
  "v_sub_f32 v182, v182, v248\n"
  "v_sub_f32 v183, v183, v248\n"
  "v_add_f32 v232, v232, v248\n"
  "v_exp_f32 v249, -v248\n"
  "s_nop 0\n"
  "v_mul_f32 v235, v235, v249\n"
  "v_max_f32 v248, v132, v133\n"
  "v_max3_f32 v248, v134, v135, v248\n"
  "v_max3_f32 v248, v172, v173, v248\n"
  "v_max3_f32 v248, v174, v175, v248\n"
  "v_max3_f32 v248, v144, v145, v248\n"
  "v_max3_f32 v248, v146, v147, v248\n"
  "v_max3_f32 v248, v184, v185, v248\n"
  "v_max3_f32 v248, v186, v187, v248\n"
  "v_mov_b32 v244, v248\n"
  "s_nop 1\n"
  "v_permlane32_swap_b32 v248, v244\n"
  "v_max_f32 v248, v248, v244\n"
  "v_mov_b32 v244, v248\n"
  "s_nop 1\n"
  "v_permlane16_swap_b32 v248, v244\n"
  "v_max_f32 v248, v248, v244\n"
  "v_ceil_f32 v248, v248\n"
  "v_max_f32 v248, 0, v248\n"
  "v_sub_f32 v132, v132, v248\n"
  "v_sub_f32 v133, v133, v248\n"
  "v_sub_f32 v134, v134, v248\n"
  "v_sub_f32 v135, v135, v248\n"
  "v_sub_f32 v172, v172, v248\n"
  "v_sub_f32 v173, v173, v248\n"
  "v_sub_f32 v174, v174, v248\n"
  "v_sub_f32 v175, v175, v248\n"
  "v_sub_f32 v144, v144, v248\n"
  "v_sub_f32 v145, v145, v248\n"
  "v_sub_f32 v146, v146, v248\n"
  "v_sub_f32 v147, v147, v248\n"
  "v_sub_f32 v184, v184, v248\n"
  "v_sub_f32 v185, v185, v248\n"
  "v_sub_f32 v186, v186, v248\n"
  "v_sub_f32 v187, v187, v248\n"
  "v_add_f32 v233, v233, v248\n"
  "v_exp_f32 v250, -v248\n"
  "s_nop 0\n"
  "v_mul_f32 v236, v236, v250\n"
  "v_max_f32 v248, v136, v137\n"
  "v_max3_f32 v248, v138, v139, v248\n"
  "v_max3_f32 v248, v176, v177, v248\n"
  "v_max3_f32 v248, v178, v179, v248\n"
  "v_mov_b32 v244, v248\n"
  "s_nop 1\n"
  "v_permlane32_swap_b32 v248, v244\n"
  "v_max_f32 v248, v248, v244\n"
  "v_mov_b32 v244, v248\n"
  "s_nop 1\n"
  "v_permlane16_swap_b32 v248, v244\n"
  "v_max_f32 v248, v248, v244\n"
  "v_ceil_f32 v248, v248\n"
  "v_max_f32 v248, 0, v248\n"
  "v_sub_f32 v136, v136, v248\n"
  "v_sub_f32 v137, v137, v248\n"
  "v_sub_f32 v138, v138, v248\n"
  "v_sub_f32 v139, v139, v248\n"
  "v_sub_f32 v176, v176, v248\n"
  "v_sub_f32 v177, v177, v248\n"
  "v_sub_f32 v178, v178, v248\n"
  "v_sub_f32 v179, v179, v248\n"
  "v_add_f32 v234, v234, v248\n"
  "v_exp_f32 v251, -v248\n"
  "s_nop 0\n"
  "v_mul_f32 v237, v237, v251\n"
  "v_mul_f32 v0, v0, v249\n"
  "v_mul_f32 v1, v1, v249\n"
  "v_mul_f32 v2, v2, v249\n"
  "v_mul_f32 v3, v3, v249\n"
  "v_mul_f32 v4, v4, v249\n"
  "v_mul_f32 v5, v5, v249\n"
  "v_mul_f32 v6, v6, v249\n"
  "v_mul_f32 v7, v7, v249\n"
  "v_mul_f32 v8, v8, v249\n"
  "v_mul_f32 v9, v9, v249\n"
  "v_mul_f32 v10, v10, v249\n"
  "v_mul_f32 v11, v11, v249\n"
  "v_mul_f32 v12, v12, v249\n"
  "v_mul_f32 v13, v13, v249\n"
  "v_mul_f32 v14, v14, v249\n"
  "v_mul_f32 v15, v15, v249\n"
  "v_mul_f32 v16, v16, v249\n"
  "v_mul_f32 v17, v17, v249\n"
  "v_mul_f32 v18, v18, v249\n"
  "v_mul_f32 v19, v19, v249\n"
  "v_mul_f32 v20, v20, v249\n"
  "v_mul_f32 v21, v21, v249\n"
  "v_mul_f32 v22, v22, v249\n"
  "v_mul_f32 v23, v23, v249\n"
  "v_mul_f32 v24, v24, v249\n"
  "v_mul_f32 v25, v25, v249\n"
  "v_mul_f32 v26, v26, v249\n"
  "v_mul_f32 v27, v27, v249\n"
  "v_mul_f32 v28, v28, v249\n"
  "v_mul_f32 v29, v29, v249\n"
  "v_mul_f32 v30, v30, v249\n"
  "v_mul_f32 v31, v31, v249\n"
  "v_mul_f32 v32, v32, v249\n"
  "v_mul_f32 v33, v33, v249\n"
  "v_mul_f32 v34, v34, v249\n"
  "v_mul_f32 v35, v35, v249\n"
  "v_mul_f32 v36, v36, v249\n"
  "v_mul_f32 v37, v37, v249\n"
  "v_mul_f32 v38, v38, v249\n"
  "v_mul_f32 v39, v39, v249\n"
  "v_mul_f32 v40, v40, v249\n"
  "v_mul_f32 v41, v41, v249\n"
  "v_mul_f32 v42, v42, v249\n"
  "v_mul_f32 v43, v43, v249\n"
  "v_mul_f32 v44, v44, v249\n"
  "v_mul_f32 v45, v45, v249\n"
  "v_mul_f32 v46, v46, v249\n"
  "v_mul_f32 v47, v47, v249\n"
  "v_mul_f32 v48, v48, v249\n"
  "v_mul_f32 v49, v49, v249\n"
  "v_mul_f32 v50, v50, v249\n"
  "v_mul_f32 v51, v51, v249\n"
  "v_mul_f32 v52, v52, v249\n"
  "v_mul_f32 v53, v53, v249\n"
  "v_mul_f32 v54, v54, v249\n"
  "v_mul_f32 v55, v55, v249\n"
  "v_mul_f32 v56, v56, v249\n"
  "v_mul_f32 v57, v57, v249\n"
  "v_mul_f32 v58, v58, v249\n"
  "v_mul_f32 v59, v59, v249\n"
  "v_mul_f32 v60, v60, v249\n"
  "v_mul_f32 v61, v61, v249\n"
  "v_mul_f32 v62, v62, v249\n"
  "v_mul_f32 v63, v63, v249\n"
  "v_mul_f32 v64, v64, v250\n"
  "v_mul_f32 v65, v65, v250\n"
  "v_mul_f32 v66, v66, v250\n"
  "v_mul_f32 v67, v67, v250\n"
  "v_mul_f32 v68, v68, v250\n"
  "v_mul_f32 v69, v69, v250\n"
  "v_mul_f32 v70, v70, v250\n"
  "v_mul_f32 v71, v71, v250\n"
  "v_mul_f32 v72, v72, v250\n"
  "v_mul_f32 v73, v73, v250\n"
  "v_mul_f32 v74, v74, v250\n"
  "v_mul_f32 v75, v75, v250\n"
  "v_mul_f32 v76, v76, v250\n"
  "v_mul_f32 v77, v77, v250\n"
  "v_mul_f32 v78, v78, v250\n"
  "v_mul_f32 v79, v79, v250\n"
  "v_mul_f32 v80, v80, v250\n"
  "v_mul_f32 v81, v81, v250\n"
  "v_mul_f32 v82, v82, v250\n"
  "v_mul_f32 v83, v83, v250\n"
  "v_mul_f32 v84, v84, v250\n"
  "v_mul_f32 v85, v85, v250\n"
  "v_mul_f32 v86, v86, v250\n"
  "v_mul_f32 v87, v87, v250\n"
  "v_mul_f32 v88, v88, v250\n"
  "v_mul_f32 v89, v89, v250\n"
  "v_mul_f32 v90, v90, v250\n"
  "v_mul_f32 v91, v91, v250\n"
  "v_mul_f32 v92, v92, v250\n"
  "v_mul_f32 v93, v93, v250\n"
  "v_mul_f32 v94, v94, v250\n"
  "v_mul_f32 v95, v95, v250\n"
  "v_mul_f32 v96, v96, v250\n"
  "v_mul_f32 v97, v97, v250\n"
  "v_mul_f32 v98, v98, v250\n"
  "v_mul_f32 v99, v99, v250\n"
  "v_mul_f32 v100, v100, v250\n"
  "v_mul_f32 v101, v101, v250\n"
  "v_mul_f32 v102, v102, v250\n"
  "v_mul_f32 v103, v103, v250\n"
  "v_mul_f32 v104, v104, v250\n"
  "v_mul_f32 v105, v105, v250\n"
  "v_mul_f32 v106, v106, v250\n"
  "v_mul_f32 v107, v107, v250\n"
  "v_mul_f32 v108, v108, v250\n"
  "v_mul_f32 v109, v109, v250\n"
  "v_mul_f32 v110, v110, v250\n"
  "v_mul_f32 v111, v111, v250\n"
  "v_mul_f32 v112, v112, v250\n"
  "v_mul_f32 v113, v113, v250\n"
  "v_mul_f32 v114, v114, v250\n"
  "v_mul_f32 v115, v115, v250\n"
  "v_mul_f32 v116, v116, v250\n"
  "v_mul_f32 v117, v117, v250\n"
  "v_mul_f32 v118, v118, v250\n"
  "v_mul_f32 v119, v119, v250\n"
  "v_mul_f32 v120, v120, v250\n"
  "v_mul_f32 v121, v121, v250\n"
  "v_mul_f32 v122, v122, v250\n"
  "v_mul_f32 v123, v123, v250\n"
  "v_mul_f32 v124, v124, v250\n"
  "v_mul_f32 v125, v125, v250\n"
  "v_mul_f32 v126, v126, v250\n"
  "v_mul_f32 v127, v127, v250\n"
  "v_accvgpr_read_b32 v244, a0\n"
  "v_mul_f32 v244, v244, v251\n"
  "v_accvgpr_write_b32 a0, v244\n"
  "v_accvgpr_read_b32 v245, a1\n"
  "v_mul_f32 v245, v245, v251\n"
  "v_accvgpr_write_b32 a1, v245\n"
  "v_accvgpr_read_b32 v246, a2\n"
  "v_mul_f32 v246, v246, v251\n"
  "v_accvgpr_write_b32 a2, v246\n"
  "v_accvgpr_read_b32 v247, a3\n"
  "v_mul_f32 v247, v247, v251\n"
  "v_accvgpr_write_b32 a3, v247\n"
  "v_accvgpr_read_b32 v244, a4\n"
  "v_mul_f32 v244, v244, v251\n"
  "v_accvgpr_write_b32 a4, v244\n"
  "v_accvgpr_read_b32 v245, a5\n"
  "v_mul_f32 v245, v245, v251\n"
  "v_accvgpr_write_b32 a5, v245\n"
  "v_accvgpr_read_b32 v246, a6\n"
  "v_mul_f32 v246, v246, v251\n"
  "v_accvgpr_write_b32 a6, v246\n"
  "v_accvgpr_read_b32 v247, a7\n"
  "v_mul_f32 v247, v247, v251\n"
  "v_accvgpr_write_b32 a7, v247\n"
  "v_accvgpr_read_b32 v244, a8\n"
  "v_mul_f32 v244, v244, v251\n"
  "v_accvgpr_write_b32 a8, v244\n"
  "v_accvgpr_read_b32 v245, a9\n"
  "v_mul_f32 v245, v245, v251\n"
  "v_accvgpr_write_b32 a9, v245\n"
  "v_accvgpr_read_b32 v246, a10\n"
  "v_mul_f32 v246, v246, v251\n"
  "v_accvgpr_write_b32 a10, v246\n"
  "v_accvgpr_read_b32 v247, a11\n"
  "v_mul_f32 v247, v247, v251\n"
  "v_accvgpr_write_b32 a11, v247\n"
  "v_accvgpr_read_b32 v244, a12\n"
  "v_mul_f32 v244, v244, v251\n"
  "v_accvgpr_write_b32 a12, v244\n"
  "v_accvgpr_read_b32 v245, a13\n"
  "v_mul_f32 v245, v245, v251\n"
  "v_accvgpr_write_b32 a13, v245\n"
  "v_accvgpr_read_b32 v246, a14\n"
  "v_mul_f32 v246, v246, v251\n"
  "v_accvgpr_write_b32 a14, v246\n"
  "v_accvgpr_read_b32 v247, a15\n"
  "v_mul_f32 v247, v247, v251\n"
  "v_accvgpr_write_b32 a15, v247\n"
  "v_accvgpr_read_b32 v244, a16\n"
  "v_mul_f32 v244, v244, v251\n"
  "v_accvgpr_write_b32 a16, v244\n"
  "v_accvgpr_read_b32 v245, a17\n"
  "v_mul_f32 v245, v245, v251\n"
  "v_accvgpr_write_b32 a17, v245\n"
  "v_accvgpr_read_b32 v246, a18\n"
  "v_mul_f32 v246, v246, v251\n"
  "v_accvgpr_write_b32 a18, v246\n"
  "v_accvgpr_read_b32 v247, a19\n"
  "v_mul_f32 v247, v247, v251\n"
  "v_accvgpr_write_b32 a19, v247\n"
  "v_accvgpr_read_b32 v244, a20\n"
  "v_mul_f32 v244, v244, v251\n"
  "v_accvgpr_write_b32 a20, v244\n"
  "v_accvgpr_read_b32 v245, a21\n"
  "v_mul_f32 v245, v245, v251\n"
  "v_accvgpr_write_b32 a21, v245\n"
  "v_accvgpr_read_b32 v246, a22\n"
  "v_mul_f32 v246, v246, v251\n"
  "v_accvgpr_write_b32 a22, v246\n"
  "v_accvgpr_read_b32 v247, a23\n"
  "v_mul_f32 v247, v247, v251\n"
  "v_accvgpr_write_b32 a23, v247\n"
  "v_accvgpr_read_b32 v244, a24\n"
  "v_mul_f32 v244, v244, v251\n"
  "v_accvgpr_write_b32 a24, v244\n"
  "v_accvgpr_read_b32 v245, a25\n"
  "v_mul_f32 v245, v245, v251\n"
  "v_accvgpr_write_b32 a25, v245\n"
  "v_accvgpr_read_b32 v246, a26\n"
  "v_mul_f32 v246, v246, v251\n"
  "v_accvgpr_write_b32 a26, v246\n"
  "v_accvgpr_read_b32 v247, a27\n"
  "v_mul_f32 v247, v247, v251\n"
  "v_accvgpr_write_b32 a27, v247\n"
  "v_accvgpr_read_b32 v244, a28\n"
  "v_mul_f32 v244, v244, v251\n"
  "v_accvgpr_write_b32 a28, v244\n"
  "v_accvgpr_read_b32 v245, a29\n"
  "v_mul_f32 v245, v245, v251\n"
  "v_accvgpr_write_b32 a29, v245\n"
  "v_accvgpr_read_b32 v246, a30\n"
  "v_mul_f32 v246, v246, v251\n"
  "v_accvgpr_write_b32 a30, v246\n"
  "v_accvgpr_read_b32 v247, a31\n"
  "v_mul_f32 v247, v247, v251\n"
  "v_accvgpr_write_b32 a31, v247\n"
  "v_accvgpr_read_b32 v244, a32\n"
  "v_mul_f32 v244, v244, v251\n"
  "v_accvgpr_write_b32 a32, v244\n"
  "v_accvgpr_read_b32 v245, a33\n"
  "v_mul_f32 v245, v245, v251\n"
  "v_accvgpr_write_b32 a33, v245\n"
  "v_accvgpr_read_b32 v246, a34\n"
  "v_mul_f32 v246, v246, v251\n"
  "v_accvgpr_write_b32 a34, v246\n"
  "v_accvgpr_read_b32 v247, a35\n"
  "v_mul_f32 v247, v247, v251\n"
  "v_accvgpr_write_b32 a35, v247\n"
  "v_accvgpr_read_b32 v244, a36\n"
  "v_mul_f32 v244, v244, v251\n"
  "v_accvgpr_write_b32 a36, v244\n"
  "v_accvgpr_read_b32 v245, a37\n"
  "v_mul_f32 v245, v245, v251\n"
  "v_accvgpr_write_b32 a37, v245\n"
  "v_accvgpr_read_b32 v246, a38\n"
  "v_mul_f32 v246, v246, v251\n"
  "v_accvgpr_write_b32 a38, v246\n"
  "v_accvgpr_read_b32 v247, a39\n"
  "v_mul_f32 v247, v247, v251\n"
  "v_accvgpr_write_b32 a39, v247\n"
  "v_accvgpr_read_b32 v244, a40\n"
  "v_mul_f32 v244, v244, v251\n"
  "v_accvgpr_write_b32 a40, v244\n"
  "v_accvgpr_read_b32 v245, a41\n"
  "v_mul_f32 v245, v245, v251\n"
  "v_accvgpr_write_b32 a41, v245\n"
  "v_accvgpr_read_b32 v246, a42\n"
  "v_mul_f32 v246, v246, v251\n"
  "v_accvgpr_write_b32 a42, v246\n"
  "v_accvgpr_read_b32 v247, a43\n"
  "v_mul_f32 v247, v247, v251\n"
  "v_accvgpr_write_b32 a43, v247\n"
  "v_accvgpr_read_b32 v244, a44\n"
  "v_mul_f32 v244, v244, v251\n"
  "v_accvgpr_write_b32 a44, v244\n"
  "v_accvgpr_read_b32 v245, a45\n"
  "v_mul_f32 v245, v245, v251\n"
  "v_accvgpr_write_b32 a45, v245\n"
  "v_accvgpr_read_b32 v246, a46\n"
  "v_mul_f32 v246, v246, v251\n"
  "v_accvgpr_write_b32 a46, v246\n"
  "v_accvgpr_read_b32 v247, a47\n"
  "v_mul_f32 v247, v247, v251\n"
  "v_accvgpr_write_b32 a47, v247\n"
  "v_accvgpr_read_b32 v244, a48\n"
  "v_mul_f32 v244, v244, v251\n"
  "v_accvgpr_write_b32 a48, v244\n"
  "v_accvgpr_read_b32 v245, a49\n"
  "v_mul_f32 v245, v245, v251\n"
  "v_accvgpr_write_b32 a49, v245\n"
  "v_accvgpr_read_b32 v246, a50\n"
  "v_mul_f32 v246, v246, v251\n"
  "v_accvgpr_write_b32 a50, v246\n"
  "v_accvgpr_read_b32 v247, a51\n"
  "v_mul_f32 v247, v247, v251\n"
  "v_accvgpr_write_b32 a51, v247\n"
  "v_accvgpr_read_b32 v244, a52\n"
  "v_mul_f32 v244, v244, v251\n"
  "v_accvgpr_write_b32 a52, v244\n"
  "v_accvgpr_read_b32 v245, a53\n"
  "v_mul_f32 v245, v245, v251\n"
  "v_accvgpr_write_b32 a53, v245\n"
  "v_accvgpr_read_b32 v246, a54\n"
  "v_mul_f32 v246, v246, v251\n"
  "v_accvgpr_write_b32 a54, v246\n"
  "v_accvgpr_read_b32 v247, a55\n"
  "v_mul_f32 v247, v247, v251\n"
  "v_accvgpr_write_b32 a55, v247\n"
  "v_accvgpr_read_b32 v244, a56\n"
  "v_mul_f32 v244, v244, v251\n"
  "v_accvgpr_write_b32 a56, v244\n"
  "v_accvgpr_read_b32 v245, a57\n"
  "v_mul_f32 v245, v245, v251\n"
  "v_accvgpr_write_b32 a57, v245\n"
  "v_accvgpr_read_b32 v246, a58\n"
  "v_mul_f32 v246, v246, v251\n"
  "v_accvgpr_write_b32 a58, v246\n"
  "v_accvgpr_read_b32 v247, a59\n"
  "v_mul_f32 v247, v247, v251\n"
  "v_accvgpr_write_b32 a59, v247\n"
  "v_accvgpr_read_b32 v244, a60\n"
  "v_mul_f32 v244, v244, v251\n"
  "v_accvgpr_write_b32 a60, v244\n"
  "v_accvgpr_read_b32 v245, a61\n"
  "v_mul_f32 v245, v245, v251\n"
  "v_accvgpr_write_b32 a61, v245\n"
  "v_accvgpr_read_b32 v246, a62\n"
  "v_mul_f32 v246, v246, v251\n"
  "v_accvgpr_write_b32 a62, v246\n"
  "v_accvgpr_read_b32 v247, a63\n"
  "v_mul_f32 v247, v247, v251\n"
  "v_accvgpr_write_b32 a63, v247\n"
  "v_exp_f32 v128, v128\n"
  "v_exp_f32 v129, v129\n"
  "v_exp_f32 v130, v130\n"
  "v_exp_f32 v131, v131\n"
  "v_exp_f32 v132, v132\n"
  "v_exp_f32 v133, v133\n"
  "v_exp_f32 v134, v134\n"
  "v_exp_f32 v135, v135\n"
  "v_exp_f32 v136, v136\n"
  "v_exp_f32 v137, v137\n"
  "v_exp_f32 v138, v138\n"
  "v_exp_f32 v139, v139\n"
  "v_exp_f32 v176, v176\n"
  "v_exp_f32 v177, v177\n"
  "v_exp_f32 v178, v178\n"
  "v_exp_f32 v179, v179\n"
  "v_exp_f32 v172, v172\n"
  "v_exp_f32 v173, v173\n"
  "v_exp_f32 v174, v174\n"
  "v_exp_f32 v175, v175\n"
  "v_exp_f32 v168, v168\n"
  "v_exp_f32 v169, v169\n"
  "v_exp_f32 v170, v170\n"
  "v_exp_f32 v171, v171\n"
  "v_exp_f32 v140, v140\n"
  "v_exp_f32 v141, v141\n"
  "v_exp_f32 v142, v142\n"
  "v_exp_f32 v143, v143\n"
  "v_exp_f32 v144, v144\n"
  "v_exp_f32 v145, v145\n"
  "v_exp_f32 v146, v146\n"
  "v_exp_f32 v147, v147\n"
  "v_exp_f32 v184, v184\n"
  "v_exp_f32 v185, v185\n"
  "v_exp_f32 v186, v186\n"
  "v_exp_f32 v187, v187\n"
  "v_exp_f32 v180, v180\n"
  "v_exp_f32 v181, v181\n"
  "v_exp_f32 v182, v182\n"
  "v_exp_f32 v183, v183\n"
  "s_nop 0\n"
  "v_add_f32 v238, v128, v129\n"
  "v_add_f32 v238, v238, v130\n"
  "v_add_f32 v238, v238, v131\n"
  "v_add_f32 v239, v132, v133\n"
  "v_add_f32 v239, v239, v134\n"
  "v_add_f32 v239, v239, v135\n"
  "v_add_f32 v240, v136, v137\n"
  "v_add_f32 v240, v240, v138\n"
  "v_add_f32 v240, v240, v139\n"
  "v_add_f32 v240, v240, v176\n"
  "v_add_f32 v240, v240, v177\n"
  "v_add_f32 v240, v240, v178\n"
  "v_add_f32 v240, v240, v179\n"
  "v_add_f32 v239, v239, v172\n"
  "v_add_f32 v239, v239, v173\n"
  "v_add_f32 v239, v239, v174\n"
  "v_add_f32 v239, v239, v175\n"
  "v_add_f32 v238, v238, v168\n"
  "v_add_f32 v238, v238, v169\n"
  "v_add_f32 v238, v238, v170\n"
  "v_add_f32 v238, v238, v171\n"
  "v_add_f32 v238, v238, v140\n"
  "v_add_f32 v238, v238, v141\n"
  "v_add_f32 v238, v238, v142\n"
  "v_add_f32 v238, v238, v143\n"
  "v_add_f32 v239, v239, v144\n"
  "v_add_f32 v239, v239, v145\n"
  "v_add_f32 v239, v239, v146\n"
  "v_add_f32 v239, v239, v147\n"
  "v_add_f32 v239, v239, v184\n"
  "v_add_f32 v239, v239, v185\n"
  "v_add_f32 v239, v239, v186\n"
  "v_add_f32 v239, v239, v187\n"
  "v_add_f32 v238, v238, v180\n"
  "v_add_f32 v238, v238, v181\n"
  "v_add_f32 v238, v238, v182\n"
  "v_add_f32 v238, v238, v183\n"
  "v_cvt_pk_bf16_f32 v128, v128, v129\n"
  "v_cvt_pk_bf16_f32 v129, v130, v131\n"
  "v_cvt_pk_bf16_f32 v132, v132, v133\n"
  "v_cvt_pk_bf16_f32 v133, v134, v135\n"
  "v_cvt_pk_bf16_f32 v136, v136, v137\n"
  "v_cvt_pk_bf16_f32 v137, v138, v139\n"
  "v_cvt_pk_bf16_f32 v138, v176, v177\n"
  "v_cvt_pk_bf16_f32 v139, v178, v179\n"
  "v_cvt_pk_bf16_f32 v134, v172, v173\n"
  "v_cvt_pk_bf16_f32 v135, v174, v175\n"
  "v_cvt_pk_bf16_f32 v130, v168, v169\n"
  "v_cvt_pk_bf16_f32 v131, v170, v171\n"
  "v_cvt_pk_bf16_f32 v140, v140, v141\n"
  "v_cvt_pk_bf16_f32 v141, v142, v143\n"
  "s_nop 1\n"
  "s_branch .Lattn_LOOP\n"
  ".Lattn_RARE1B:\n"
  "s_nop 7\n"
  "s_mov_b32 s84, s90\n"
  "s_mov_b32 s85, s91\n"
  "s_mov_b32 s86, 1048576\n"
  "s_mov_b32 s87, 0x20000\n"
  "s_sub_u32 s64, s58, 1\n"
  "s_lshl_b32 s64, s64, 15\n"
  "s_add_u32 s64, s64, s77\n"
  "s_and_b32 s64, s64, 0x7ffff\n"
  "s_or_b32 s64, s64, s78\n"
  "s_lshl_b32 s65, s70, 11\n"
  "v_mbcnt_lo_u32_b32 v244, -1, 0\n"
  "v_mbcnt_hi_u32_b32 v244, -1, v244\n"
  "v_and_b32 v245, 15, v244\n"
  "v_and_b32 v247, 9, v245\n"
  "v_bfe_u32 v246, v245, 1, 2\n"
  "v_lshl_add_u32 v247, v246, 4, v247\n"
  "v_lshlrev_b32 v247, 9, v247\n"
  "v_lshrrev_b32 v246, 4, v244\n"
  "v_lshl_add_u32 v247, v246, 4, v247\n"
  "v_add_u32 v247, s65, v247\n"
  "v_xor_b32 v248, 0x800, v247\n"
  "s_nop 7\n"
  "buffer_load_dwordx4 a[224:227], v247, s[84:87], s64 offen offset:0\n"
  "buffer_load_dwordx4 a[228:231], v247, s[84:87], s64 offen offset:64\n"
  "buffer_load_dwordx4 a[232:235], v247, s[84:87], s64 offen offset:128\n"
  "buffer_load_dwordx4 a[236:239], v247, s[84:87], s64 offen offset:192\n"
  "buffer_load_dwordx4 a[240:243], v247, s[84:87], s64 offen offset:256\n"
  "buffer_load_dwordx4 a[244:247], v247, s[84:87], s64 offen offset:320\n"
  "buffer_load_dwordx4 a[248:251], v247, s[84:87], s64 offen offset:384\n"
  "buffer_load_dwordx4 a[252:255], v247, s[84:87], s64 offen offset:448\n"
  "s_waitcnt vmcnt(0)\n"
  "v_mfma_f32_16x16x32_bf16 v[148:151], a[224:227], a[64:67], 0\n"
  "v_mfma_f32_16x16x32_bf16 v[148:151], a[228:231], a[68:71], v[148:151]\n"
  "v_mfma_f32_16x16x32_bf16 v[148:151], a[232:235], a[72:75], v[148:151]\n"
  "v_mfma_f32_16x16x32_bf16 v[148:151], a[236:239], a[76:79], v[148:151]\n"
  "v_mfma_f32_16x16x32_bf16 v[148:151], a[240:243], a[80:83], v[148:151]\n"
  "v_mfma_f32_16x16x32_bf16 v[148:151], a[244:247], a[84:87], v[148:151]\n"
  "v_mfma_f32_16x16x32_bf16 v[148:151], a[248:251], a[88:91], v[148:151]\n"
  "v_mfma_f32_16x16x32_bf16 v[148:151], a[252:255], a[92:95], v[148:151]\n"
  "v_mfma_f32_16x16x32_bf16 v[152:155], a[224:227], a[96:99], 0\n"
  "v_mfma_f32_16x16x32_bf16 v[152:155], a[228:231], a[100:103], v[152:155]\n"
  "v_mfma_f32_16x16x32_bf16 v[152:155], a[232:235], a[104:107], v[152:155]\n"
  "v_mfma_f32_16x16x32_bf16 v[152:155], a[236:239], a[108:111], v[152:155]\n"
  "v_mfma_f32_16x16x32_bf16 v[152:155], a[240:243], a[112:115], v[152:155]\n"
  "v_mfma_f32_16x16x32_bf16 v[152:155], a[244:247], a[116:119], v[152:155]\n"
  "v_mfma_f32_16x16x32_bf16 v[152:155], a[248:251], a[120:123], v[152:155]\n"
  "v_mfma_f32_16x16x32_bf16 v[152:155], a[252:255], a[124:127], v[152:155]\n"
  "v_mfma_f32_16x16x32_bf16 v[156:159], a[224:227], a[128:131], 0\n"
  "v_mfma_f32_16x16x32_bf16 v[156:159], a[228:231], a[132:135], v[156:159]\n"
  "v_mfma_f32_16x16x32_bf16 v[156:159], a[232:235], a[136:139], v[156:159]\n"
  "v_mfma_f32_16x16x32_bf16 v[156:159], a[236:239], a[140:143], v[156:159]\n"
  "v_mfma_f32_16x16x32_bf16 v[156:159], a[240:243], a[144:147], v[156:159]\n"
  "v_mfma_f32_16x16x32_bf16 v[156:159], a[244:247], a[148:151], v[156:159]\n"
  "v_mfma_f32_16x16x32_bf16 v[156:159], a[248:251], a[152:155], v[156:159]\n"
  "v_mfma_f32_16x16x32_bf16 v[156:159], a[252:255], a[156:159], v[156:159]\n"
  "s_nop 7\n"
  "buffer_load_dwordx4 a[224:227], v247, s[84:87], s64 offen offset:1024\n"
  "buffer_load_dwordx4 a[228:231], v247, s[84:87], s64 offen offset:1088\n"
  "buffer_load_dwordx4 a[232:235], v247, s[84:87], s64 offen offset:1152\n"
  "buffer_load_dwordx4 a[236:239], v247, s[84:87], s64 offen offset:1216\n"
  "buffer_load_dwordx4 a[240:243], v247, s[84:87], s64 offen offset:1280\n"
  "buffer_load_dwordx4 a[244:247], v247, s[84:87], s64 offen offset:1344\n"
  "buffer_load_dwordx4 a[248:251], v247, s[84:87], s64 offen offset:1408\n"
  "buffer_load_dwordx4 a[252:255], v247, s[84:87], s64 offen offset:1472\n"
  "s_waitcnt vmcnt(0)\n"
  "v_mfma_f32_16x16x32_bf16 v[176:179], a[224:227], a[128:131], 0\n"
  "v_mfma_f32_16x16x32_bf16 v[176:179], a[228:231], a[132:135], v[176:179]\n"
  "v_mfma_f32_16x16x32_bf16 v[176:179], a[232:235], a[136:139], v[176:179]\n"
  "v_mfma_f32_16x16x32_bf16 v[176:179], a[236:239], a[140:143], v[176:179]\n"
  "v_mfma_f32_16x16x32_bf16 v[176:179], a[240:243], a[144:147], v[176:179]\n"
  "v_mfma_f32_16x16x32_bf16 v[176:179], a[244:247], a[148:151], v[176:179]\n"
  "v_mfma_f32_16x16x32_bf16 v[176:179], a[248:251], a[152:155], v[176:179]\n"
  "v_mfma_f32_16x16x32_bf16 v[176:179], a[252:255], a[156:159], v[176:179]\n"
  "v_mfma_f32_16x16x32_bf16 v[172:175], a[224:227], a[96:99], 0\n"
  "v_mfma_f32_16x16x32_bf16 v[172:175], a[228:231], a[100:103], v[172:175]\n"
  "v_mfma_f32_16x16x32_bf16 v[172:175], a[232:235], a[104:107], v[172:175]\n"
  "v_mfma_f32_16x16x32_bf16 v[172:175], a[236:239], a[108:111], v[172:175]\n"
  "v_mfma_f32_16x16x32_bf16 v[172:175], a[240:243], a[112:115], v[172:175]\n"
  "v_mfma_f32_16x16x32_bf16 v[172:175], a[244:247], a[116:119], v[172:175]\n"
  "v_mfma_f32_16x16x32_bf16 v[172:175], a[248:251], a[120:123], v[172:175]\n"
  "v_mfma_f32_16x16x32_bf16 v[172:175], a[252:255], a[124:127], v[172:175]\n"
  "v_mfma_f32_16x16x32_bf16 v[168:171], a[224:227], a[64:67], 0\n"
  "v_mfma_f32_16x16x32_bf16 v[168:171], a[228:231], a[68:71], v[168:171]\n"
  "v_mfma_f32_16x16x32_bf16 v[168:171], a[232:235], a[72:75], v[168:171]\n"
  "v_mfma_f32_16x16x32_bf16 v[168:171], a[236:239], a[76:79], v[168:171]\n"
  "v_mfma_f32_16x16x32_bf16 v[168:171], a[240:243], a[80:83], v[168:171]\n"
  "v_mfma_f32_16x16x32_bf16 v[168:171], a[244:247], a[84:87], v[168:171]\n"
  "v_mfma_f32_16x16x32_bf16 v[168:171], a[248:251], a[88:91], v[168:171]\n"
  "v_mfma_f32_16x16x32_bf16 v[168:171], a[252:255], a[92:95], v[168:171]\n"
  "s_nop 7\n"
  "buffer_load_dwordx4 a[224:227], v248, s[84:87], s64 offen offset:0\n"
  "buffer_load_dwordx4 a[228:231], v248, s[84:87], s64 offen offset:64\n"
  "buffer_load_dwordx4 a[232:235], v248, s[84:87], s64 offen offset:128\n"
  "buffer_load_dwordx4 a[236:239], v248, s[84:87], s64 offen offset:192\n"
  "buffer_load_dwordx4 a[240:243], v248, s[84:87], s64 offen offset:256\n"
  "buffer_load_dwordx4 a[244:247], v248, s[84:87], s64 offen offset:320\n"
  "buffer_load_dwordx4 a[248:251], v248, s[84:87], s64 offen offset:384\n"
  "buffer_load_dwordx4 a[252:255], v248, s[84:87], s64 offen offset:448\n"
  "s_waitcnt vmcnt(0)\n"
  "v_mfma_f32_16x16x32_bf16 v[160:163], a[224:227], a[64:67], 0\n"
  "v_mfma_f32_16x16x32_bf16 v[160:163], a[228:231], a[68:71], v[160:163]\n"
  "v_mfma_f32_16x16x32_bf16 v[160:163], a[232:235], a[72:75], v[160:163]\n"
  "v_mfma_f32_16x16x32_bf16 v[160:163], a[236:239], a[76:79], v[160:163]\n"
  "v_mfma_f32_16x16x32_bf16 v[160:163], a[240:243], a[80:83], v[160:163]\n"
  "v_mfma_f32_16x16x32_bf16 v[160:163], a[244:247], a[84:87], v[160:163]\n"
  "v_mfma_f32_16x16x32_bf16 v[160:163], a[248:251], a[88:91], v[160:163]\n"
  "v_mfma_f32_16x16x32_bf16 v[160:163], a[252:255], a[92:95], v[160:163]\n"
  "v_mfma_f32_16x16x32_bf16 v[164:167], a[224:227], a[96:99], 0\n"
  "v_mfma_f32_16x16x32_bf16 v[164:167], a[228:231], a[100:103], v[164:167]\n"
  "v_mfma_f32_16x16x32_bf16 v[164:167], a[232:235], a[104:107], v[164:167]\n"
  "v_mfma_f32_16x16x32_bf16 v[164:167], a[236:239], a[108:111], v[164:167]\n"
  "v_mfma_f32_16x16x32_bf16 v[164:167], a[240:243], a[112:115], v[164:167]\n"
  "v_mfma_f32_16x16x32_bf16 v[164:167], a[244:247], a[116:119], v[164:167]\n"
  "v_mfma_f32_16x16x32_bf16 v[164:167], a[248:251], a[120:123], v[164:167]\n"
  "v_mfma_f32_16x16x32_bf16 v[164:167], a[252:255], a[124:127], v[164:167]\n"
  "s_nop 7\n"
  "buffer_load_dwordx4 a[224:227], v248, s[84:87], s64 offen offset:1024\n"
  "buffer_load_dwordx4 a[228:231], v248, s[84:87], s64 offen offset:1088\n"
  "buffer_load_dwordx4 a[232:235], v248, s[84:87], s64 offen offset:1152\n"
  "buffer_load_dwordx4 a[236:239], v248, s[84:87], s64 offen offset:1216\n"
  "buffer_load_dwordx4 a[240:243], v248, s[84:87], s64 offen offset:1280\n"
  "buffer_load_dwordx4 a[244:247], v248, s[84:87], s64 offen offset:1344\n"
  "buffer_load_dwordx4 a[248:251], v248, s[84:87], s64 offen offset:1408\n"
  "buffer_load_dwordx4 a[252:255], v248, s[84:87], s64 offen offset:1472\n"
  "s_waitcnt vmcnt(0)\n"
  "v_mfma_f32_16x16x32_bf16 v[184:187], a[224:227], a[96:99], 0\n"
  "v_mfma_f32_16x16x32_bf16 v[184:187], a[228:231], a[100:103], v[184:187]\n"
  "v_mfma_f32_16x16x32_bf16 v[184:187], a[232:235], a[104:107], v[184:187]\n"
  "v_mfma_f32_16x16x32_bf16 v[184:187], a[236:239], a[108:111], v[184:187]\n"
  "v_mfma_f32_16x16x32_bf16 v[184:187], a[240:243], a[112:115], v[184:187]\n"
  "v_mfma_f32_16x16x32_bf16 v[184:187], a[244:247], a[116:119], v[184:187]\n"
  "v_mfma_f32_16x16x32_bf16 v[184:187], a[248:251], a[120:123], v[184:187]\n"
  "v_mfma_f32_16x16x32_bf16 v[184:187], a[252:255], a[124:127], v[184:187]\n"
  "v_mfma_f32_16x16x32_bf16 v[180:183], a[224:227], a[64:67], 0\n"
  "v_mfma_f32_16x16x32_bf16 v[180:183], a[228:231], a[68:71], v[180:183]\n"
  "v_mfma_f32_16x16x32_bf16 v[180:183], a[232:235], a[72:75], v[180:183]\n"
  "v_mfma_f32_16x16x32_bf16 v[180:183], a[236:239], a[76:79], v[180:183]\n"
  "v_mfma_f32_16x16x32_bf16 v[180:183], a[240:243], a[80:83], v[180:183]\n"
  "v_mfma_f32_16x16x32_bf16 v[180:183], a[244:247], a[84:87], v[180:183]\n"
  "v_mfma_f32_16x16x32_bf16 v[180:183], a[248:251], a[88:91], v[180:183]\n"
  "v_mfma_f32_16x16x32_bf16 v[180:183], a[252:255], a[92:95], v[180:183]\n"
  "s_nop 7\n"
  "v_fma_f32 v148, v148, s56, -v232\n"
  "v_fma_f32 v149, v149, s56, -v232\n"
  "v_fma_f32 v150, v150, s56, -v232\n"
  "v_fma_f32 v151, v151, s56, -v232\n"
  "v_fma_f32 v152, v152, s56, -v233\n"
  "v_fma_f32 v153, v153, s56, -v233\n"
  "v_fma_f32 v154, v154, s56, -v233\n"
  "v_fma_f32 v155, v155, s56, -v233\n"
  "v_fma_f32 v156, v156, s56, -v234\n"
  "v_fma_f32 v157, v157, s56, -v234\n"
  "v_fma_f32 v158, v158, s56, -v234\n"
  "v_fma_f32 v159, v159, s56, -v234\n"
  "v_fma_f32 v176, v176, s56, -v234\n"
  "v_fma_f32 v177, v177, s56, -v234\n"
  "v_fma_f32 v178, v178, s56, -v234\n"
  "v_fma_f32 v179, v179, s56, -v234\n"
  "v_fma_f32 v172, v172, s56, -v233\n"
  "v_fma_f32 v173, v173, s56, -v233\n"
  "v_fma_f32 v174, v174, s56, -v233\n"
  "v_fma_f32 v175, v175, s56, -v233\n"
  "v_fma_f32 v168, v168, s56, -v232\n"
  "v_fma_f32 v169, v169, s56, -v232\n"
  "v_fma_f32 v170, v170, s56, -v232\n"
  "v_fma_f32 v171, v171, s56, -v232\n"
  "v_fma_f32 v160, v160, s56, -v232\n"
  "v_fma_f32 v161, v161, s56, -v232\n"
  "v_fma_f32 v162, v162, s56, -v232\n"
  "v_fma_f32 v163, v163, s56, -v232\n"
  "v_fma_f32 v164, v164, s56, -v233\n"
  "v_fma_f32 v165, v165, s56, -v233\n"
  "v_fma_f32 v166, v166, s56, -v233\n"
  "v_fma_f32 v167, v167, s56, -v233\n"
  "v_fma_f32 v184, v184, s56, -v233\n"
  "v_fma_f32 v185, v185, s56, -v233\n"
  "v_fma_f32 v186, v186, s56, -v233\n"
  "v_fma_f32 v187, v187, s56, -v233\n"
  "v_fma_f32 v180, v180, s56, -v232\n"
  "v_fma_f32 v181, v181, s56, -v232\n"
  "v_fma_f32 v182, v182, s56, -v232\n"
  "v_fma_f32 v183, v183, s56, -v232\n"
  "v_max_f32 v248, v148, v149\n"
  "v_max3_f32 v248, v150, v151, v248\n"
  "v_max3_f32 v248, v168, v169, v248\n"
  "v_max3_f32 v248, v170, v171, v248\n"
  "v_max3_f32 v248, v160, v161, v248\n"
  "v_max3_f32 v248, v162, v163, v248\n"
  "v_max3_f32 v248, v180, v181, v248\n"
  "v_max3_f32 v248, v182, v183, v248\n"
  "v_mov_b32 v244, v248\n"
  "s_nop 1\n"
  "v_permlane32_swap_b32 v248, v244\n"
  "v_max_f32 v248, v248, v244\n"
  "v_mov_b32 v244, v248\n"
  "s_nop 1\n"
  "v_permlane16_swap_b32 v248, v244\n"
  "v_max_f32 v248, v248, v244\n"
  "v_ceil_f32 v248, v248\n"
  "v_max_f32 v248, 0, v248\n"
  "v_sub_f32 v148, v148, v248\n"
  "v_sub_f32 v149, v149, v248\n"
  "v_sub_f32 v150, v150, v248\n"
  "v_sub_f32 v151, v151, v248\n"
  "v_sub_f32 v168, v168, v248\n"
  "v_sub_f32 v169, v169, v248\n"
  "v_sub_f32 v170, v170, v248\n"
  "v_sub_f32 v171, v171, v248\n"
  "v_sub_f32 v160, v160, v248\n"
  "v_sub_f32 v161, v161, v248\n"
  "v_sub_f32 v162, v162, v248\n"
  "v_sub_f32 v163, v163, v248\n"
  "v_sub_f32 v180, v180, v248\n"
  "v_sub_f32 v181, v181, v248\n"
  "v_sub_f32 v182, v182, v248\n"
  "v_sub_f32 v183, v183, v248\n"
  "v_add_f32 v232, v232, v248\n"
  "v_exp_f32 v249, -v248\n"
  "s_nop 0\n"
  "v_mul_f32 v235, v235, v249\n"
  "v_max_f32 v248, v152, v153\n"
  "v_max3_f32 v248, v154, v155, v248\n"
  "v_max3_f32 v248, v172, v173, v248\n"
  "v_max3_f32 v248, v174, v175, v248\n"
  "v_max3_f32 v248, v164, v165, v248\n"
  "v_max3_f32 v248, v166, v167, v248\n"
  "v_max3_f32 v248, v184, v185, v248\n"
  "v_max3_f32 v248, v186, v187, v248\n"
  "v_mov_b32 v244, v248\n"
  "s_nop 1\n"
  "v_permlane32_swap_b32 v248, v244\n"
  "v_max_f32 v248, v248, v244\n"
  "v_mov_b32 v244, v248\n"
  "s_nop 1\n"
  "v_permlane16_swap_b32 v248, v244\n"
  "v_max_f32 v248, v248, v244\n"
  "v_ceil_f32 v248, v248\n"
  "v_max_f32 v248, 0, v248\n"
  "v_sub_f32 v152, v152, v248\n"
  "v_sub_f32 v153, v153, v248\n"
  "v_sub_f32 v154, v154, v248\n"
  "v_sub_f32 v155, v155, v248\n"
  "v_sub_f32 v172, v172, v248\n"
  "v_sub_f32 v173, v173, v248\n"
  "v_sub_f32 v174, v174, v248\n"
  "v_sub_f32 v175, v175, v248\n"
  "v_sub_f32 v164, v164, v248\n"
  "v_sub_f32 v165, v165, v248\n"
  "v_sub_f32 v166, v166, v248\n"
  "v_sub_f32 v167, v167, v248\n"
  "v_sub_f32 v184, v184, v248\n"
  "v_sub_f32 v185, v185, v248\n"
  "v_sub_f32 v186, v186, v248\n"
  "v_sub_f32 v187, v187, v248\n"
  "v_add_f32 v233, v233, v248\n"
  "v_exp_f32 v250, -v248\n"
  "s_nop 0\n"
  "v_mul_f32 v236, v236, v250\n"
  "v_max_f32 v248, v156, v157\n"
  "v_max3_f32 v248, v158, v159, v248\n"
  "v_max3_f32 v248, v176, v177, v248\n"
  "v_max3_f32 v248, v178, v179, v248\n"
  "v_mov_b32 v244, v248\n"
  "s_nop 1\n"
  "v_permlane32_swap_b32 v248, v244\n"
  "v_max_f32 v248, v248, v244\n"
  "v_mov_b32 v244, v248\n"
  "s_nop 1\n"
  "v_permlane16_swap_b32 v248, v244\n"
  "v_max_f32 v248, v248, v244\n"
  "v_ceil_f32 v248, v248\n"
  "v_max_f32 v248, 0, v248\n"
  "v_sub_f32 v156, v156, v248\n"
  "v_sub_f32 v157, v157, v248\n"
  "v_sub_f32 v158, v158, v248\n"
  "v_sub_f32 v159, v159, v248\n"
  "v_sub_f32 v176, v176, v248\n"
  "v_sub_f32 v177, v177, v248\n"
  "v_sub_f32 v178, v178, v248\n"
  "v_sub_f32 v179, v179, v248\n"
  "v_add_f32 v234, v234, v248\n"
  "v_exp_f32 v251, -v248\n"
  "s_nop 0\n"
  "v_mul_f32 v237, v237, v251\n"
  "v_mul_f32 v0, v0, v249\n"
  "v_mul_f32 v1, v1, v249\n"
  "v_mul_f32 v2, v2, v249\n"
  "v_mul_f32 v3, v3, v249\n"
  "v_mul_f32 v4, v4, v249\n"
  "v_mul_f32 v5, v5, v249\n"
  "v_mul_f32 v6, v6, v249\n"
  "v_mul_f32 v7, v7, v249\n"
  "v_mul_f32 v8, v8, v249\n"
  "v_mul_f32 v9, v9, v249\n"
  "v_mul_f32 v10, v10, v249\n"
  "v_mul_f32 v11, v11, v249\n"
  "v_mul_f32 v12, v12, v249\n"
  "v_mul_f32 v13, v13, v249\n"
  "v_mul_f32 v14, v14, v249\n"
  "v_mul_f32 v15, v15, v249\n"
  "v_mul_f32 v16, v16, v249\n"
  "v_mul_f32 v17, v17, v249\n"
  "v_mul_f32 v18, v18, v249\n"
  "v_mul_f32 v19, v19, v249\n"
  "v_mul_f32 v20, v20, v249\n"
  "v_mul_f32 v21, v21, v249\n"
  "v_mul_f32 v22, v22, v249\n"
  "v_mul_f32 v23, v23, v249\n"
  "v_mul_f32 v24, v24, v249\n"
  "v_mul_f32 v25, v25, v249\n"
  "v_mul_f32 v26, v26, v249\n"
  "v_mul_f32 v27, v27, v249\n"
  "v_mul_f32 v28, v28, v249\n"
  "v_mul_f32 v29, v29, v249\n"
  "v_mul_f32 v30, v30, v249\n"
  "v_mul_f32 v31, v31, v249\n"
  "v_mul_f32 v32, v32, v249\n"
  "v_mul_f32 v33, v33, v249\n"
  "v_mul_f32 v34, v34, v249\n"
  "v_mul_f32 v35, v35, v249\n"
  "v_mul_f32 v36, v36, v249\n"
  "v_mul_f32 v37, v37, v249\n"
  "v_mul_f32 v38, v38, v249\n"
  "v_mul_f32 v39, v39, v249\n"
  "v_mul_f32 v40, v40, v249\n"
  "v_mul_f32 v41, v41, v249\n"
  "v_mul_f32 v42, v42, v249\n"
  "v_mul_f32 v43, v43, v249\n"
  "v_mul_f32 v44, v44, v249\n"
  "v_mul_f32 v45, v45, v249\n"
  "v_mul_f32 v46, v46, v249\n"
  "v_mul_f32 v47, v47, v249\n"
  "v_mul_f32 v48, v48, v249\n"
  "v_mul_f32 v49, v49, v249\n"
  "v_mul_f32 v50, v50, v249\n"
  "v_mul_f32 v51, v51, v249\n"
  "v_mul_f32 v52, v52, v249\n"
  "v_mul_f32 v53, v53, v249\n"
  "v_mul_f32 v54, v54, v249\n"
  "v_mul_f32 v55, v55, v249\n"
  "v_mul_f32 v56, v56, v249\n"
  "v_mul_f32 v57, v57, v249\n"
  "v_mul_f32 v58, v58, v249\n"
  "v_mul_f32 v59, v59, v249\n"
  "v_mul_f32 v60, v60, v249\n"
  "v_mul_f32 v61, v61, v249\n"
  "v_mul_f32 v62, v62, v249\n"
  "v_mul_f32 v63, v63, v249\n"
  "v_mul_f32 v64, v64, v250\n"
  "v_mul_f32 v65, v65, v250\n"
  "v_mul_f32 v66, v66, v250\n"
  "v_mul_f32 v67, v67, v250\n"
  "v_mul_f32 v68, v68, v250\n"
  "v_mul_f32 v69, v69, v250\n"
  "v_mul_f32 v70, v70, v250\n"
  "v_mul_f32 v71, v71, v250\n"
  "v_mul_f32 v72, v72, v250\n"
  "v_mul_f32 v73, v73, v250\n"
  "v_mul_f32 v74, v74, v250\n"
  "v_mul_f32 v75, v75, v250\n"
  "v_mul_f32 v76, v76, v250\n"
  "v_mul_f32 v77, v77, v250\n"
  "v_mul_f32 v78, v78, v250\n"
  "v_mul_f32 v79, v79, v250\n"
  "v_mul_f32 v80, v80, v250\n"
  "v_mul_f32 v81, v81, v250\n"
  "v_mul_f32 v82, v82, v250\n"
  "v_mul_f32 v83, v83, v250\n"
  "v_mul_f32 v84, v84, v250\n"
  "v_mul_f32 v85, v85, v250\n"
  "v_mul_f32 v86, v86, v250\n"
  "v_mul_f32 v87, v87, v250\n"
  "v_mul_f32 v88, v88, v250\n"
  "v_mul_f32 v89, v89, v250\n"
  "v_mul_f32 v90, v90, v250\n"
  "v_mul_f32 v91, v91, v250\n"
  "v_mul_f32 v92, v92, v250\n"
  "v_mul_f32 v93, v93, v250\n"
  "v_mul_f32 v94, v94, v250\n"
  "v_mul_f32 v95, v95, v250\n"
  "v_mul_f32 v96, v96, v250\n"
  "v_mul_f32 v97, v97, v250\n"
  "v_mul_f32 v98, v98, v250\n"
  "v_mul_f32 v99, v99, v250\n"
  "v_mul_f32 v100, v100, v250\n"
  "v_mul_f32 v101, v101, v250\n"
  "v_mul_f32 v102, v102, v250\n"
  "v_mul_f32 v103, v103, v250\n"
  "v_mul_f32 v104, v104, v250\n"
  "v_mul_f32 v105, v105, v250\n"
  "v_mul_f32 v106, v106, v250\n"
  "v_mul_f32 v107, v107, v250\n"
  "v_mul_f32 v108, v108, v250\n"
  "v_mul_f32 v109, v109, v250\n"
  "v_mul_f32 v110, v110, v250\n"
  "v_mul_f32 v111, v111, v250\n"
  "v_mul_f32 v112, v112, v250\n"
  "v_mul_f32 v113, v113, v250\n"
  "v_mul_f32 v114, v114, v250\n"
  "v_mul_f32 v115, v115, v250\n"
  "v_mul_f32 v116, v116, v250\n"
  "v_mul_f32 v117, v117, v250\n"
  "v_mul_f32 v118, v118, v250\n"
  "v_mul_f32 v119, v119, v250\n"
  "v_mul_f32 v120, v120, v250\n"
  "v_mul_f32 v121, v121, v250\n"
  "v_mul_f32 v122, v122, v250\n"
  "v_mul_f32 v123, v123, v250\n"
  "v_mul_f32 v124, v124, v250\n"
  "v_mul_f32 v125, v125, v250\n"
  "v_mul_f32 v126, v126, v250\n"
  "v_mul_f32 v127, v127, v250\n"
  "v_accvgpr_read_b32 v244, a0\n"
  "v_mul_f32 v244, v244, v251\n"
  "v_accvgpr_write_b32 a0, v244\n"
  "v_accvgpr_read_b32 v245, a1\n"
  "v_mul_f32 v245, v245, v251\n"
  "v_accvgpr_write_b32 a1, v245\n"
  "v_accvgpr_read_b32 v246, a2\n"
  "v_mul_f32 v246, v246, v251\n"
  "v_accvgpr_write_b32 a2, v246\n"
  "v_accvgpr_read_b32 v247, a3\n"
  "v_mul_f32 v247, v247, v251\n"
  "v_accvgpr_write_b32 a3, v247\n"
  "v_accvgpr_read_b32 v244, a4\n"
  "v_mul_f32 v244, v244, v251\n"
  "v_accvgpr_write_b32 a4, v244\n"
  "v_accvgpr_read_b32 v245, a5\n"
  "v_mul_f32 v245, v245, v251\n"
  "v_accvgpr_write_b32 a5, v245\n"
  "v_accvgpr_read_b32 v246, a6\n"
  "v_mul_f32 v246, v246, v251\n"
  "v_accvgpr_write_b32 a6, v246\n"
  "v_accvgpr_read_b32 v247, a7\n"
  "v_mul_f32 v247, v247, v251\n"
  "v_accvgpr_write_b32 a7, v247\n"
  "v_accvgpr_read_b32 v244, a8\n"
  "v_mul_f32 v244, v244, v251\n"
  "v_accvgpr_write_b32 a8, v244\n"
  "v_accvgpr_read_b32 v245, a9\n"
  "v_mul_f32 v245, v245, v251\n"
  "v_accvgpr_write_b32 a9, v245\n"
  "v_accvgpr_read_b32 v246, a10\n"
  "v_mul_f32 v246, v246, v251\n"
  "v_accvgpr_write_b32 a10, v246\n"
  "v_accvgpr_read_b32 v247, a11\n"
  "v_mul_f32 v247, v247, v251\n"
  "v_accvgpr_write_b32 a11, v247\n"
  "v_accvgpr_read_b32 v244, a12\n"
  "v_mul_f32 v244, v244, v251\n"
  "v_accvgpr_write_b32 a12, v244\n"
  "v_accvgpr_read_b32 v245, a13\n"
  "v_mul_f32 v245, v245, v251\n"
  "v_accvgpr_write_b32 a13, v245\n"
  "v_accvgpr_read_b32 v246, a14\n"
  "v_mul_f32 v246, v246, v251\n"
  "v_accvgpr_write_b32 a14, v246\n"
  "v_accvgpr_read_b32 v247, a15\n"
  "v_mul_f32 v247, v247, v251\n"
  "v_accvgpr_write_b32 a15, v247\n"
  "v_accvgpr_read_b32 v244, a16\n"
  "v_mul_f32 v244, v244, v251\n"
  "v_accvgpr_write_b32 a16, v244\n"
  "v_accvgpr_read_b32 v245, a17\n"
  "v_mul_f32 v245, v245, v251\n"
  "v_accvgpr_write_b32 a17, v245\n"
  "v_accvgpr_read_b32 v246, a18\n"
  "v_mul_f32 v246, v246, v251\n"
  "v_accvgpr_write_b32 a18, v246\n"
  "v_accvgpr_read_b32 v247, a19\n"
  "v_mul_f32 v247, v247, v251\n"
  "v_accvgpr_write_b32 a19, v247\n"
  "v_accvgpr_read_b32 v244, a20\n"
  "v_mul_f32 v244, v244, v251\n"
  "v_accvgpr_write_b32 a20, v244\n"
  "v_accvgpr_read_b32 v245, a21\n"
  "v_mul_f32 v245, v245, v251\n"
  "v_accvgpr_write_b32 a21, v245\n"
  "v_accvgpr_read_b32 v246, a22\n"
  "v_mul_f32 v246, v246, v251\n"
  "v_accvgpr_write_b32 a22, v246\n"
  "v_accvgpr_read_b32 v247, a23\n"
  "v_mul_f32 v247, v247, v251\n"
  "v_accvgpr_write_b32 a23, v247\n"
  "v_accvgpr_read_b32 v244, a24\n"
  "v_mul_f32 v244, v244, v251\n"
  "v_accvgpr_write_b32 a24, v244\n"
  "v_accvgpr_read_b32 v245, a25\n"
  "v_mul_f32 v245, v245, v251\n"
  "v_accvgpr_write_b32 a25, v245\n"
  "v_accvgpr_read_b32 v246, a26\n"
  "v_mul_f32 v246, v246, v251\n"
  "v_accvgpr_write_b32 a26, v246\n"
  "v_accvgpr_read_b32 v247, a27\n"
  "v_mul_f32 v247, v247, v251\n"
  "v_accvgpr_write_b32 a27, v247\n"
  "v_accvgpr_read_b32 v244, a28\n"
  "v_mul_f32 v244, v244, v251\n"
  "v_accvgpr_write_b32 a28, v244\n"
  "v_accvgpr_read_b32 v245, a29\n"
  "v_mul_f32 v245, v245, v251\n"
  "v_accvgpr_write_b32 a29, v245\n"
  "v_accvgpr_read_b32 v246, a30\n"
  "v_mul_f32 v246, v246, v251\n"
  "v_accvgpr_write_b32 a30, v246\n"
  "v_accvgpr_read_b32 v247, a31\n"
  "v_mul_f32 v247, v247, v251\n"
  "v_accvgpr_write_b32 a31, v247\n"
  "v_accvgpr_read_b32 v244, a32\n"
  "v_mul_f32 v244, v244, v251\n"
  "v_accvgpr_write_b32 a32, v244\n"
  "v_accvgpr_read_b32 v245, a33\n"
  "v_mul_f32 v245, v245, v251\n"
  "v_accvgpr_write_b32 a33, v245\n"
  "v_accvgpr_read_b32 v246, a34\n"
  "v_mul_f32 v246, v246, v251\n"
  "v_accvgpr_write_b32 a34, v246\n"
  "v_accvgpr_read_b32 v247, a35\n"
  "v_mul_f32 v247, v247, v251\n"
  "v_accvgpr_write_b32 a35, v247\n"
  "v_accvgpr_read_b32 v244, a36\n"
  "v_mul_f32 v244, v244, v251\n"
  "v_accvgpr_write_b32 a36, v244\n"
  "v_accvgpr_read_b32 v245, a37\n"
  "v_mul_f32 v245, v245, v251\n"
  "v_accvgpr_write_b32 a37, v245\n"
  "v_accvgpr_read_b32 v246, a38\n"
  "v_mul_f32 v246, v246, v251\n"
  "v_accvgpr_write_b32 a38, v246\n"
  "v_accvgpr_read_b32 v247, a39\n"
  "v_mul_f32 v247, v247, v251\n"
  "v_accvgpr_write_b32 a39, v247\n"
  "v_accvgpr_read_b32 v244, a40\n"
  "v_mul_f32 v244, v244, v251\n"
  "v_accvgpr_write_b32 a40, v244\n"
  "v_accvgpr_read_b32 v245, a41\n"
  "v_mul_f32 v245, v245, v251\n"
  "v_accvgpr_write_b32 a41, v245\n"
  "v_accvgpr_read_b32 v246, a42\n"
  "v_mul_f32 v246, v246, v251\n"
  "v_accvgpr_write_b32 a42, v246\n"
  "v_accvgpr_read_b32 v247, a43\n"
  "v_mul_f32 v247, v247, v251\n"
  "v_accvgpr_write_b32 a43, v247\n"
  "v_accvgpr_read_b32 v244, a44\n"
  "v_mul_f32 v244, v244, v251\n"
  "v_accvgpr_write_b32 a44, v244\n"
  "v_accvgpr_read_b32 v245, a45\n"
  "v_mul_f32 v245, v245, v251\n"
  "v_accvgpr_write_b32 a45, v245\n"
  "v_accvgpr_read_b32 v246, a46\n"
  "v_mul_f32 v246, v246, v251\n"
  "v_accvgpr_write_b32 a46, v246\n"
  "v_accvgpr_read_b32 v247, a47\n"
  "v_mul_f32 v247, v247, v251\n"
  "v_accvgpr_write_b32 a47, v247\n"
  "v_accvgpr_read_b32 v244, a48\n"
  "v_mul_f32 v244, v244, v251\n"
  "v_accvgpr_write_b32 a48, v244\n"
  "v_accvgpr_read_b32 v245, a49\n"
  "v_mul_f32 v245, v245, v251\n"
  "v_accvgpr_write_b32 a49, v245\n"
  "v_accvgpr_read_b32 v246, a50\n"
  "v_mul_f32 v246, v246, v251\n"
  "v_accvgpr_write_b32 a50, v246\n"
  "v_accvgpr_read_b32 v247, a51\n"
  "v_mul_f32 v247, v247, v251\n"
  "v_accvgpr_write_b32 a51, v247\n"
  "v_accvgpr_read_b32 v244, a52\n"
  "v_mul_f32 v244, v244, v251\n"
  "v_accvgpr_write_b32 a52, v244\n"
  "v_accvgpr_read_b32 v245, a53\n"
  "v_mul_f32 v245, v245, v251\n"
  "v_accvgpr_write_b32 a53, v245\n"
  "v_accvgpr_read_b32 v246, a54\n"
  "v_mul_f32 v246, v246, v251\n"
  "v_accvgpr_write_b32 a54, v246\n"
  "v_accvgpr_read_b32 v247, a55\n"
  "v_mul_f32 v247, v247, v251\n"
  "v_accvgpr_write_b32 a55, v247\n"
  "v_accvgpr_read_b32 v244, a56\n"
  "v_mul_f32 v244, v244, v251\n"
  "v_accvgpr_write_b32 a56, v244\n"
  "v_accvgpr_read_b32 v245, a57\n"
  "v_mul_f32 v245, v245, v251\n"
  "v_accvgpr_write_b32 a57, v245\n"
  "v_accvgpr_read_b32 v246, a58\n"
  "v_mul_f32 v246, v246, v251\n"
  "v_accvgpr_write_b32 a58, v246\n"
  "v_accvgpr_read_b32 v247, a59\n"
  "v_mul_f32 v247, v247, v251\n"
  "v_accvgpr_write_b32 a59, v247\n"
  "v_accvgpr_read_b32 v244, a60\n"
  "v_mul_f32 v244, v244, v251\n"
  "v_accvgpr_write_b32 a60, v244\n"
  "v_accvgpr_read_b32 v245, a61\n"
  "v_mul_f32 v245, v245, v251\n"
  "v_accvgpr_write_b32 a61, v245\n"
  "v_accvgpr_read_b32 v246, a62\n"
  "v_mul_f32 v246, v246, v251\n"
  "v_accvgpr_write_b32 a62, v246\n"
  "v_accvgpr_read_b32 v247, a63\n"
  "v_mul_f32 v247, v247, v251\n"
  "v_accvgpr_write_b32 a63, v247\n"
  "v_exp_f32 v148, v148\n"
  "v_exp_f32 v149, v149\n"
  "v_exp_f32 v150, v150\n"
  "v_exp_f32 v151, v151\n"
  "v_exp_f32 v152, v152\n"
  "v_exp_f32 v153, v153\n"
  "v_exp_f32 v154, v154\n"
  "v_exp_f32 v155, v155\n"
  "v_exp_f32 v156, v156\n"
  "v_exp_f32 v157, v157\n"
  "v_exp_f32 v158, v158\n"
  "v_exp_f32 v159, v159\n"
  "v_exp_f32 v176, v176\n"
  "v_exp_f32 v177, v177\n"
  "v_exp_f32 v178, v178\n"
  "v_exp_f32 v179, v179\n"
  "v_exp_f32 v172, v172\n"
  "v_exp_f32 v173, v173\n"
  "v_exp_f32 v174, v174\n"
  "v_exp_f32 v175, v175\n"
  "v_exp_f32 v168, v168\n"
  "v_exp_f32 v169, v169\n"
  "v_exp_f32 v170, v170\n"
  "v_exp_f32 v171, v171\n"
  "v_exp_f32 v160, v160\n"
  "v_exp_f32 v161, v161\n"
  "v_exp_f32 v162, v162\n"
  "v_exp_f32 v163, v163\n"
  "v_exp_f32 v164, v164\n"
  "v_exp_f32 v165, v165\n"
  "v_exp_f32 v166, v166\n"
  "v_exp_f32 v167, v167\n"
  "v_exp_f32 v184, v184\n"
  "v_exp_f32 v185, v185\n"
  "v_exp_f32 v186, v186\n"
  "v_exp_f32 v187, v187\n"
  "v_exp_f32 v180, v180\n"
  "v_exp_f32 v181, v181\n"
  "v_exp_f32 v182, v182\n"
  "v_exp_f32 v183, v183\n"
  "s_nop 0\n"
  "v_add_f32 v238, v148, v149\n"
  "v_add_f32 v238, v238, v150\n"
  "v_add_f32 v238, v238, v151\n"
  "v_add_f32 v239, v152, v153\n"
  "v_add_f32 v239, v239, v154\n"
  "v_add_f32 v239, v239, v155\n"
  "v_add_f32 v240, v156, v157\n"
  "v_add_f32 v240, v240, v158\n"
  "v_add_f32 v240, v240, v159\n"
  "v_add_f32 v240, v240, v176\n"
  "v_add_f32 v240, v240, v177\n"
  "v_add_f32 v240, v240, v178\n"
  "v_add_f32 v240, v240, v179\n"
  "v_add_f32 v239, v239, v172\n"
  "v_add_f32 v239, v239, v173\n"
  "v_add_f32 v239, v239, v174\n"
  "v_add_f32 v239, v239, v175\n"
  "v_add_f32 v238, v238, v168\n"
  "v_add_f32 v238, v238, v169\n"
  "v_add_f32 v238, v238, v170\n"
  "v_add_f32 v238, v238, v171\n"
  "v_add_f32 v238, v238, v160\n"
  "v_add_f32 v238, v238, v161\n"
  "v_add_f32 v238, v238, v162\n"
  "v_add_f32 v238, v238, v163\n"
  "v_add_f32 v239, v239, v164\n"
  "v_add_f32 v239, v239, v165\n"
  "v_add_f32 v239, v239, v166\n"
  "v_add_f32 v239, v239, v167\n"
  "v_add_f32 v239, v239, v184\n"
  "v_add_f32 v239, v239, v185\n"
  "v_add_f32 v239, v239, v186\n"
  "v_add_f32 v239, v239, v187\n"
  "v_add_f32 v238, v238, v180\n"
  "v_add_f32 v238, v238, v181\n"
  "v_add_f32 v238, v238, v182\n"
  "v_add_f32 v238, v238, v183\n"
  "v_cvt_pk_bf16_f32 v148, v148, v149\n"
  "v_cvt_pk_bf16_f32 v149, v150, v151\n"
  "v_cvt_pk_bf16_f32 v152, v152, v153\n"
  "v_cvt_pk_bf16_f32 v153, v154, v155\n"
  "v_cvt_pk_bf16_f32 v156, v156, v157\n"
  "v_cvt_pk_bf16_f32 v157, v158, v159\n"
  "v_cvt_pk_bf16_f32 v158, v176, v177\n"
  "v_cvt_pk_bf16_f32 v159, v178, v179\n"
  "v_cvt_pk_bf16_f32 v154, v172, v173\n"
  "v_cvt_pk_bf16_f32 v155, v174, v175\n"
  "v_cvt_pk_bf16_f32 v150, v168, v169\n"
  "v_cvt_pk_bf16_f32 v151, v170, v171\n"
  "v_cvt_pk_bf16_f32 v160, v160, v161\n"
  "v_cvt_pk_bf16_f32 v161, v162, v163\n"
  "s_nop 1\n"
  "s_bitcmp1_b32 s69, s58\n"
  "s_cbranch_scc0 .Lattn_RET1\n"
  "s_branch .Lattn_FINAL\n"
  ".Lattn_DONE:\n"
  :: "s"((unsigned)pq), "s"((unsigned)(pq >> 32)), "s"((unsigned)pk), "s"((unsigned)(pk >> 32)), "s"((unsigned)pv), "s"((unsigned)(pv >> 32)),
     "s"((unsigned)po), "s"((unsigned)(po >> 32)), "s"(blockIdx.x), "s"(__builtin_amdgcn_readfirstlane(threadIdx.x >> 6))
  : "v0", "v1", "v2", "v3", "v4", "v5", "v6", "v7", "v8", "v9", "v10", "v11", "v12", "v13", "v14", "v15", "v16", "v17", "v18", "v19", "v20", "v21", "v22", "v23", "v24", "v25", "v26", "v27", "v28", "v29", "v30", "v31", "v32", "v33", "v34", "v35", "v36", "v37", "v38", "v39", "v40", "v41", "v42", "v43", "v44", "v45", "v46", "v47", "v48", "v49", "v50", "v51", "v52", "v53", "v54", "v55", "v56", "v57", "v58", "v59", "v60", "v61", "v62", "v63", "v64", "v65", "v66", "v67", "v68", "v69", "v70", "v71", "v72", "v73", "v74", "v75", "v76", "v77", "v78", "v79", "v80", "v81", "v82", "v83", "v84", "v85", "v86", "v87", "v88", "v89", "v90", "v91", "v92", "v93", "v94", "v95", "v96", "v97", "v98", "v99", "v100", "v101", "v102", "v103", "v104", "v105", "v106", "v107", "v108", "v109", "v110", "v111", "v112", "v113", "v114", "v115", "v116", "v117", "v118", "v119", "v120", "v121", "v122", "v123", "v124", "v125", "v126", "v127", "v128", "v129", "v130", "v131", "v132", "v133", "v134", "v135", "v136", "v137", "v138", "v139", "v140", "v141", "v142", "v143", "v144", "v145", "v146", "v147", "v148", "v149", "v150", "v151", "v152", "v153", "v154", "v155", "v156", "v157", "v158", "v159", "v160", "v161", "v162", "v163", "v164", "v165", "v166", "v167", "v168", "v169", "v170", "v171", "v172", "v173", "v174", "v175", "v176", "v177", "v178", "v179", "v180", "v181", "v182", "v183", "v184", "v185", "v186", "v187", "v188", "v189", "v190", "v191", "v192", "v193", "v194", "v195", "v196", "v197", "v198", "v199", "v200", "v201", "v202", "v203", "v204", "v205", "v206", "v207", "v208", "v209", "v210", "v211", "v212", "v213", "v214", "v215", "v216", "v217", "v218", "v219", "v220", "v221", "v222", "v223", "v224", "v225", "v226", "v227", "v228", "v229", "v230", "v231", "v232", "v233", "v234", "v235", "v236", "v237", "v238", "v239", "v240", "v241", "v242", "v243", "v244", "v245", "v246", "v247", "v248", "v249", "v250", "v251", "v252", "v253", "v254", "v255", "a0", "a1", "a2", "a3", "a4", "a5", "a6", "a7", "a8", "a9", "a10", "a11", "a12", "a13", "a14", "a15", "a16", "a17", "a18", "a19", "a20", "a21", "a22", "a23", "a24", "a25", "a26", "a27", "a28", "a29", "a30", "a31", "a32", "a33", "a34", "a35", "a36", "a37", "a38", "a39", "a40", "a41", "a42", "a43", "a44", "a45", "a46", "a47", "a48", "a49", "a50", "a51", "a52", "a53", "a54", "a55", "a56", "a57", "a58", "a59", "a60", "a61", "a62", "a63", "a64", "a65", "a66", "a67", "a68", "a69", "a70", "a71", "a72", "a73", "a74", "a75", "a76", "a77", "a78", "a79", "a80", "a81", "a82", "a83", "a84", "a85", "a86", "a87", "a88", "a89", "a90", "a91", "a92", "a93", "a94", "a95", "a96", "a97", "a98", "a99", "a100", "a101", "a102", "a103", "a104", "a105", "a106", "a107", "a108", "a109", "a110", "a111", "a112", "a113", "a114", "a115", "a116", "a117", "a118", "a119", "a120", "a121", "a122", "a123", "a124", "a125", "a126", "a127", "a128", "a129", "a130", "a131", "a132", "a133", "a134", "a135", "a136", "a137", "a138", "a139", "a140", "a141", "a142", "a143", "a144", "a145", "a146", "a147", "a148", "a149", "a150", "a151", "a152", "a153", "a154", "a155", "a156", "a157", "a158", "a159", "a160", "a161", "a162", "a163", "a164", "a165", "a166", "a167", "a168", "a169", "a170", "a171", "a172", "a173", "a174", "a175", "a176", "a177", "a178", "a179", "a180", "a181", "a182", "a183", "a184", "a185", "a186", "a187", "a188", "a189", "a190", "a191", "a192", "a193", "a194", "a195", "a196", "a197", "a198", "a199", "a200", "a201", "a202", "a203", "a204", "a205", "a206", "a207", "a208", "a209", "a210", "a211", "a212", "a213", "a214", "a215", "a216", "a217", "a218", "a219", "a220", "a221", "a222", "a223", "a224", "a225", "a226", "a227", "a228", "a229", "a230", "a231", "a232", "a233", "a234", "a235", "a236", "a237", "a238", "a239", "a240", "a241", "a242", "a243", "a244", "a245", "a246", "a247", "a248", "a249", "a250", "a251", "a252", "a253", "a254", "a255", "s20", "s21", "s22", "s23", "s24", "s25", "s26", "s27", "s40", "s41", "s42", "s43", "s44", "s45", "s46", "s47", "s48", "s49", "s50", "s51", "s52", "s53", "s54", "s55", "s56", "s57", "s58", "s59", "s60", "s61", "s62", "s63", "s64", "s65", "s66", "s67", "s68", "s69", "s70", "s71", "s72", "s73", "s74", "s75", "s76", "s77", "s78", "s79", "s80", "s81", "s82", "s83", "s84", "s85", "s86", "s87", "s88", "s89", "s90", "s91", "s92", "s93", "s94", "s95", "s96", "s97", "s98", "s99", "s100", "s101", "vcc", "scc", "memory");
}

extern "C" void kernel_launch(void* const* d_in, const int* in_sizes, int n_in, void* d_out, int out_size, void* d_ws, size_t ws_size, hipStream_t stream) {
  (void)in_sizes; (void)n_in; (void)out_size; (void)d_ws; (void)ws_size;
  mha_fwd_d256_kernel<<<256, 256, 0, stream>>>(d_in[0], d_in[1], d_in[2], d_out);
}
```
